```python
import torch

def _softmax(x, axis=-1):
    return torch.softmax(x, dim=axis)

def _gelu(x):
    return torch.nn.functional.gelu(x, approximate='tanh')

def _silu(x):
    return torch.nn.functional.silu(x)

def _sigmoid(x):
    return torch.sigmoid(x)

def _layernorm(x, axis=-1, eps=1e-05):
    m = x.mean(dim=axis, keepdim=True)
    v = x.var(dim=axis, keepdim=True, unbiased=False)
    return (x - m) / torch.sqrt(v + eps)

def _scatter_add(tgt, idx, src, axis=0):
    return tgt.clone().index_add_(axis, idx, src)

def _segment_sum(src, seg, n):
    out = torch.zeros((n,) + src.shape[1:], dtype=src.dtype)
    return out.index_add_(0, seg, src)

def _pad(x, pw):
    flat = tuple((v for pair in reversed(pw) for v in pair))
    return torch.nn.functional.pad(x, flat)
kernel_name = 'standalone__conformer_family_branchformer__speech'

def setup_inputs():

    _w = lambda a: (lambda t: t.to(torch.bfloat16) if t.is_floating_point() else t)(torch.as_tensor(a))
    B, T, D, K = (2, 4000, 144, 3)
    s = float(D) ** (-0.5)
    return {'x': _w((lambda *s: torch.randn(*s, dtype=torch.bfloat16) * 0.05)(B, T, D)), 'w_q': _w((lambda *s: torch.randn(*s, dtype=torch.bfloat16) * 1.6)(D, D) * s), 'w_k': _w((lambda *s: torch.randn(*s, dtype=torch.bfloat16) * 1.6)(D, D) * s), 'w_v': _w((lambda *s: torch.randn(*s, dtype=torch.bfloat16) * 0.4)(D, D) * s), 'w_ao': _w((lambda *s: torch.randn(*s, dtype=torch.bfloat16) * 0.4)(D, D) * s), 'w_up': _w((lambda *s: torch.randn(*s, dtype=torch.bfloat16) * 0.8)(D, 2 * D) * s), 'dw': _w((lambda *s: torch.randn(*s, dtype=torch.bfloat16) * 0.8)(K, D) * (1.0 / K)), 'w_dn': _w((lambda *s: torch.randn(*s, dtype=torch.bfloat16) * 0.4)(D, D) * s), 'w_merge': _w((lambda *s: torch.randn(*s, dtype=torch.bfloat16) * 0.4)(2 * D, D) * s)}

def reference(x, w_q, w_k, w_v, w_ao, w_up, dw, w_dn, w_merge):
    B, T, D = x.shape
    K = dw.shape[0]
    xa = _layernorm(x, axis=-1)
    q = torch.einsum('btd,de->bte', xa, w_q)
    k = torch.einsum('btd,de->bte', xa, w_k)
    v = torch.einsum('btd,de->bte', xa, w_v)
    logits = torch.einsum('btd,bsd->bts', q, k) * float(D) ** (-0.5)
    attn = _softmax(logits, axis=-1)
    ya = torch.einsum('bts,bsd->btd', attn, v)
    ya = torch.einsum('btd,de->bte', ya, w_ao)
    xc = _layernorm(x, axis=-1)
    h = _gelu(torch.einsum('btd,de->bte', xc, w_up))
    u = h[..., :D].clone()
    g = _layernorm(h[..., D:], axis=-1)
    pad = K // 2
    gp = _pad(g, ((0, 0), (pad, pad), (0, 0)))
    conv = torch.zeros_like(g)
    for i in range(K):
        conv = conv + gp[:, i:i + T, :] * dw[i][None, None, :]
    yc = torch.einsum('btd,de->bte', u * conv, w_dn)
    merged = torch.concatenate([ya, yc], axis=-1)
    out = torch.einsum('btf,fd->btd', merged, w_merge)
    return x + out
```

```cpp
#include <hip/hip_runtime.h>
#include <hip/hip_bf16.h>
typedef unsigned short u16;
constexpr int B=2,T=4000,D=144,R=B*T;
__device__ inline float bf2f(u16 v){return __uint_as_float(((unsigned)v)<<16);}
__device__ inline u16 f2bf(float f){unsigned u=__float_as_uint(f);u+=0x7fffu+((u>>16)&1u);return (u16)(u>>16);}
__global__ void k_ln(const u16* x,float* xa){int r=blockIdx.x*blockDim.x+threadIdx.x;if(r>=R)return;float m=0;for(int d=0;d<D;d++)m+=bf2f(x[r*D+d]);m/=D;float v=0;for(int d=0;d<D;d++){float t=bf2f(x[r*D+d])-m;v+=t*t;}v/=D;float rs=1.f/sqrtf(v+1e-5f);for(int d=0;d<D;d++)xa[r*D+d]=(bf2f(x[r*D+d])-m)*rs;}
__global__ void k_gemm(const float* A,int lda,const u16* W,int ldw,float* C,int ldc,int K,int N,int act,int accum){int i=blockIdx.x*blockDim.x+threadIdx.x;if(i>=R*N)return;int r=i/N,n=i%N;float s=0;for(int k=0;k<K;k++)s+=A[r*lda+k]*bf2f(W[k*ldw+n]);if(act){float u=0.7978845608f*(s+0.044715f*s*s*s);s=0.5f*s*(1.f+tanhf(u));}if(accum)s+=C[r*ldc+n];C[r*ldc+n]=s;}
__global__ void k_attn(const float* q,const float* k,const float* v,float* o){
  __shared__ float p[T];__shared__ float qs[D];__shared__ float red[256];
  int r=blockIdx.x,b=r/T,t=threadIdx.x;if(t<D)qs[t]=q[r*D+t];__syncthreads();
  float mx=-1e30f;for(int j=t;j<T;j+=256){const float* kr=k+(b*T+j)*D;float s=0;for(int d=0;d<D;d++)s+=qs[d]*kr[d];s*=(1.f/12.f);p[j]=s;mx=fmaxf(mx,s);}
  red[t]=mx;__syncthreads();for(int s=128;s;s>>=1){if(t<s)red[t]=fmaxf(red[t],red[t+s]);__syncthreads();}mx=red[0];__syncthreads();
  float l=0;for(int j=t;j<T;j+=256){float e=__expf(p[j]-mx);p[j]=e;l+=e;}red[t]=l;__syncthreads();for(int s=128;s;s>>=1){if(t<s)red[t]+=red[t+s];__syncthreads();}l=red[0];
  if(t<D){float a=0;for(int j=0;j<T;j++)a+=p[j]*v[(b*T+j)*D+t];o[r*D+t]=a/l;}
}
__global__ void k_lng(float* h){int r=blockIdx.x*blockDim.x+threadIdx.x;if(r>=R)return;float* g=h+r*2*D+D;float m=0;for(int d=0;d<D;d++)m+=g[d];m/=D;float v=0;for(int d=0;d<D;d++){float t=g[d]-m;v+=t*t;}v/=D;float rs=1.f/sqrtf(v+1e-5f);for(int d=0;d<D;d++)g[d]=(g[d]-m)*rs;}
__global__ void k_conv(const float* h,const u16* dw,float* uc){int i=blockIdx.x*blockDim.x+threadIdx.x;if(i>=R*D)return;int r=i/D,d=i%D,t=r%T;float c=0;for(int j=0;j<3;j++){int tt=t+j-1;if(tt>=0&&tt<T)c+=h[(r+j-1)*2*D+D+d]*bf2f(dw[j*D+d]);}uc[i]=h[r*2*D+d]*c;}
__global__ void k_out(const u16* x,const float* o,u16* out){int i=blockIdx.x*blockDim.x+threadIdx.x;if(i>=R*D)return;out[i]=f2bf(bf2f(x[i])+o[i]);}
extern "C" void kernel_launch(void* const* d_in,const int* in_sizes,int n_in,void* d_out,int out_size,void* d_ws,size_t ws_size,hipStream_t stream){
  const u16* x=(const u16*)d_in[0];const u16 *wq=(const u16*)d_in[1],*wk=(const u16*)d_in[2],*wv=(const u16*)d_in[3],*wao=(const u16*)d_in[4],*wup=(const u16*)d_in[5],*dw=(const u16*)d_in[6],*wdn=(const u16*)d_in[7],*wm=(const u16*)d_in[8];
  float* w=(float*)d_ws;size_t S=(size_t)R*D;float *xa=w,*q=w+S,*k=w+2*S,*v=w+3*S,*o=w+4*S,*ya=w+5*S,*h=w+6*S,*uc=w+8*S,*yc=w+9*S,*oo=w+10*S;
  int g1=(R*D+255)/256,g2=(R*2*D+255)/256;
  k_ln<<<(R+63)/64,64,0,stream>>>(x,xa);
  k_gemm<<<g1,256,0,stream>>>(xa,D,wq,D,q,D,D,D,0,0);k_gemm<<<g1,256,0,stream>>>(xa,D,wk,D,k,D,D,D,0,0);k_gemm<<<g1,256,0,stream>>>(xa,D,wv,D,v,D,D,D,0,0);
  k_attn<<<R,256,0,stream>>>(q,k,v,o);
  k_gemm<<<g1,256,0,stream>>>(o,D,wao,D,ya,D,D,D,0,0);
  k_gemm<<<g2,256,0,stream>>>(xa,D,wup,2*D,h,2*D,D,2*D,1,0);
  k_lng<<<(R+63)/64,64,0,stream>>>(h);
  k_conv<<<g1,256,0,stream>>>(h,dw,uc);
  k_gemm<<<g1,256,0,stream>>>(uc,D,wdn,D,yc,D,D,D,0,0);
  k_gemm<<<g1,256,0,stream>>>(ya,D,wm,D,oo,D,D,D,0,0);
  k_gemm<<<g1,256,0,stream>>>(yc,D,wm+D*D,D,oo,D,D,D,0,1);
  k_out<<<g1,256,0,stream>>>(x,oo,(u16*)d_out);
}
```

```cpp
#include <hip/hip_runtime.h>
#include <hip/hip_bf16.h>

typedef unsigned short u16;
typedef unsigned long long u64;
typedef __bf16 bf16x2 __attribute__((ext_vector_type(2)));
typedef __bf16 bf16x8 __attribute__((ext_vector_type(8)));
typedef float f32x2 __attribute__((ext_vector_type(2)));
typedef float f32x4 __attribute__((ext_vector_type(4)));
typedef unsigned u32x2 __attribute__((ext_vector_type(2)));
typedef unsigned u32x4 __attribute__((ext_vector_type(4)));
#define GAS __attribute__((address_space(1)))
#define DI __device__ __forceinline__
#define RLX __ATOMIC_RELAXED, __HIP_MEMORY_SCOPE_AGENT

constexpr int T = 4000, D = 144, NT = 125;
constexpr unsigned FS = 64, SPIN_CAP = 262144;
constexpr unsigned WS_FY = 4096;
constexpr unsigned WS_FW = 135168;
constexpr unsigned WS_FD = 143360;
constexpr unsigned WS_Y = 262144;
constexpr unsigned WS_IN = 2566144;
constexpr unsigned WS_WU = 5130240;
constexpr unsigned WS_WF = 5213184;
constexpr unsigned WS_END = 5296128;

constexpr unsigned LDS_BYTES = 163840;

#define MAIN_ASM \
  "v_mbcnt_lo_u32_b32 v255, -1, 0\n" \
  "s_cmp_ge_u32 %[w], 2\n" \
  "v_mbcnt_hi_u32_b32 v255, -1, v255\n" \
  "s_cselect_b32 s62, %[toc], %[tpc]\n" \
  "s_and_b32 s66, %[w], 1\n" \
  "v_and_b32 v254, 15, v255\n" \
  "s_lshl_b32 s62, s62, 5\n" \
  "s_lshl_b32 s64, s66, 4\n" \
  "v_lshrrev_b32 v253, 4, v255\n" \
  "s_add_u32 s62, s62, s64\n" \
  "s_add_u32 s62, s62, %[bT]\n" \
  "v_mul_u32_u24 v252, 0x120, v254\n" \
  "s_mul_i32 s62, s62, 0x120\n" \
  "s_mov_b64 s[58:59], %[x]\n" \
  "v_and_b32 v250, 1, v253\n" \
  "s_add_u32 s58, s58, s62\n" \
  "s_addc_u32 s59, s59, 0\n" \
  "v_lshl_add_u32 v251, v253, 4, v252\n" \
  "v_lshl_add_u32 v249, v250, 4, v252\n" \
  "s_mov_b64 s[44:45], %[ws]\n" \
  "v_lshlrev_b32 v227, 4, v255\n" \
  "s_cmp_ge_u32 %[w], 2\n" \
  "s_cbranch_scc1 PUB%=\n" \
  "v_mov_b32 v5, v249\n" \
  "v_mov_b32 v249, 0\n" \
  "s_mov_b64 s[36:37], s[58:59]\n" \
  "global_load_dwordx2 v[188:189], v249, s[44:45] sc1\n" \
  "s_cmp_eq_u32 %[w], 0\n" \
  "v_add_u32 v0, 128, v227\n" \
  "s_cselect_b64 s[52:53], %[wq], %[wk]\n" \
  "s_cselect_b64 s[68:69], -1, 0\n" \
  "s_mov_b32 s63, 82944\n" \
  "s_cselect_b32 s63, 36864, s63\n" \
  "s_cselect_b32 s62, 24, 16\n" \
  "s_cselect_b32 s65, 40, 32\n" \
  "v_xor_b32 v2, 16, v0\n" \
  "v_mov_b32 v4, 2147418112\n" \
  "v_cmp_gt_u32 vcc, s62, v255\n" \
  "s_sub_u32 s52, s52, 128\n" \
  "v_cndmask_b32 v2, v2, v227, s[68:69]\n" \
  "s_subb_u32 s53, s53, 0\n" \
  "s_and_b32 s53, s53, 0xffff\n" \
  "v_cndmask_b32 v1, v2, v4, vcc\n" \
  "v_cmp_gt_u32 vcc, 16, v255\n" \
  "v_add_u32 v3, 0x1000, v2\n" \
  "s_mov_b32 s54, 41600\n" \
  "v_cndmask_b32 v1, v1, v0, vcc\n" \
  "v_cmp_gt_u32 vcc, s65, v255\n" \
  "s_mov_b32 s55, 0x20000\n" \
  "s_mov_b32 s57, s63\n" \
  "v_cndmask_b32 v3, v4, v3, vcc\n" \
  "s_lshr_b32 s56, %[wg], 3\n" \
  "s_mul_i32 s65, s56, 29\n" \
  "s_lshr_b32 s65, s65, 8\n" \
  "s_mul_i32 s65, s65, 9\n" \
  "s_sub_u32 s56, s56, s65\n" \
  "s_mul_i32 s65, s56, 5120\n" \
  "s_add_u32 m0, s65, s57\n" \
  "s_mul_i32 s65, s56, 4608\n" \
  "s_add_u32 s56, s56, 1\n" \
  "s_cmp_eq_u32 s56, 9\n" \
  "s_cselect_b32 s56, 0, s56\n" \
  "buffer_load_dwordx4 v0, s[52:55], s65 offen offset:0 lds\n" \
  "s_and_b32 s45, s45, 0xffff\n" \
  "s_mov_b32 s46, 5296128\n" \
  "s_mov_b32 s47, 0x20000\n" \
  "s_lshr_b32 s67, %[w], 1\n" \
  "s_mul_i32 s42, %[w], 9216\n" \
  "v_bfe_u32 v164, v255, 2, 2\n" \
  "v_bfe_u32 v165, v255, 1, 1\n" \
  "buffer_load_dwordx4 v0, s[52:55], s65 offen offset:1024 lds\n" \
  "v_and_b32 v166, 1, v255\n" \
  "v_lshrrev_b32 v167, 1, v253\n" \
  "v_mul_u32_u24 v168, 0x120, v164\n" \
  "v_mul_u32_u24 v169, 2432, v250\n" \
  "v_lshl_add_u32 v168, v165, 4, v168\n" \
  "v_mul_u32_u24 v170, 5120, v167\n" \
  "buffer_load_dwordx4 v1, s[52:55], s65 offen offset:2048 lds\n" \
  "v_lshl_add_u32 v168, v166, 3, v168\n" \
  "v_lshrrev_b32 v171, 3, v254\n" \
  "v_add3_u32 v181, v168, v169, 0\n" \
  "v_xor_b32 v171, v171, v167\n" \
  "v_add_u32 v181, 36864, v181\n" \
  "v_lshl_add_u32 v171, v171, 4, v252\n" \
  "buffer_load_dwordx4 v2, s[52:55], s65 offen offset:3072 lds\n" \
  "v_add_u32 v180, v181, v170\n" \
  "v_lshl_add_u32 v171, v250, 3, v171\n" \
  "v_lshrrev_b32 v168, 1, v255\n" \
  "v_add_u32 v182, 82944, v171\n" \
  "s_sub_u32 s62, %[t1], 1\n" \
  "v_add_u32 v168, %[t0], v168\n" \
  "s_add_u32 m0, m0, 0x1000\n" \
  "s_nop 0\n" \
  "buffer_load_dwordx4 v3, s[52:55], s65 offen offset:0 lds\n" \
  "v_mul_u32_u24 v172, 12, v250\n" \
  "v_min_u32 v168, s62, v168\n" \
  "v_lshlrev_b32 v173, 2, v167\n" \
  "v_add_u32 v168, %[tb], v168\n" \
  "v_xor_b32 v170, v173, v254\n" \
  "v_lshl_add_u32 v168, v168, 1, v166\n" \
  "s_mul_i32 s65, s56, 5120\n" \
  "s_add_u32 m0, s65, s57\n" \
  "s_mul_i32 s65, s56, 4608\n" \
  "s_add_u32 s56, s56, 1\n" \
  "s_cmp_eq_u32 s56, 9\n" \
  "s_cselect_b32 s56, 0, s56\n" \
  "buffer_load_dwordx4 v0, s[52:55], s65 offen offset:0 lds\n" \
  "v_xor_b32 v170, v170, v172\n" \
  "v_mul_u32_u24 v168, 64, v168\n" \
  "v_lshlrev_b32 v170, 4, v170\n" \
  "v_add_u32 v177, 4096, v168\n" \
  "v_lshl_add_u32 v170, v250, 8, v170\n" \
  "v_add_u32 v173, v173, v164\n" \
  "buffer_load_dwordx4 v0, s[52:55], s65 offen offset:1024 lds\n" \
  "v_lshl_add_u32 v170, v167, 9, v170\n" \
  "v_lshlrev_b32 v171, 2, v165\n" \
  "v_add_u32 v228, s42, v170\n" \
  "v_xor_b32 v173, v173, v171\n" \
  "v_add_u32 v224, 0, v228\n" \
  "v_xor_b32 v173, v173, v172\n" \
  "buffer_load_dwordx4 v1, s[52:55], s65 offen offset:2048 lds\n" \
  "v_add_u32 v191, 129024, v227\n" \
  "v_lshlrev_b32 v173, 4, v173\n" \
  "v_lshl_add_u32 v168, v167, 5, v254\n" \
  "v_lshl_add_u32 v173, v166, 3, v173\n" \
  "v_lshlrev_b32 v168, 4, v168\n" \
  "v_lshl_add_u32 v173, v250, 8, v173\n" \
  "buffer_load_dwordx4 v2, s[52:55], s65 offen offset:3072 lds\n" \
  "v_lshl_add_u32 v168, v250, 3, v168\n" \
  "v_lshl_add_u32 v173, v165, 9, v173\n" \
  "s_mul_i32 s62, s67, 9216\n" \
  "v_sub_u32 v229, v173, v170\n" \
  "s_lshl_b32 s63, s66, 8\n" \
  "v_xor_b32 v173, 0x80, v173\n" \
  "s_add_u32 m0, m0, 0x1000\n" \
  "s_nop 0\n" \
  "buffer_load_dwordx4 v3, s[52:55], s65 offen offset:0 lds\n" \
  "s_add_u32 s62, s62, s63\n" \
  "v_sub_u32 v230, v173, v170\n" \
  "s_add_u32 s62, s62, 129024\n" \
  "v_lshlrev_b32 v169, 2, v250\n" \
  "s_mul_i32 s63, s66, 12\n" \
  "v_xor_b32 v169, v169, v254\n" \
  "v_add_u32 v183, s62, v168\n" \
  "s_mul_i32 s65, s56, 5120\n" \
  "s_add_u32 m0, s65, s57\n" \
  "s_mul_i32 s65, s56, 4608\n" \
  "s_add_u32 s56, s56, 1\n" \
  "s_cmp_eq_u32 s56, 9\n" \
  "s_cselect_b32 s56, 0, s56\n" \
  "buffer_load_dwordx4 v0, s[52:55], s65 offen offset:0 lds\n" \
  "v_xor_b32 v169, s63, v169\n" \
  "v_mov_b32 v184, 0x3f803f80\n" \
  "v_lshlrev_b32 v169, 4, v169\n" \
  "v_mov_b32 v185, 0.006944444444444444\n" \
  "v_lshl_add_u32 v176, v253, 9, v169\n" \
  "v_add_u32 v231, 0x1000, v227\n" \
  "buffer_load_dwordx4 v0, s[52:55], s65 offen offset:1024 lds\n" \
  "v_add_u32 v248, 0x2000, v227\n" \
  "v_lshrrev_b32 v164, 2, v254\n" \
  "v_and_b32 v165, 1, v254\n" \
  "v_cmp_eq_u32 vcc, v164, v253\n" \
  "v_lshlrev_b32 v165, 4, v165\n" \
  "v_mov_b32 v166, 0x3f80\n" \
  "buffer_load_dwordx4 v1, s[52:55], s65 offen offset:2048 lds\n" \
  "v_lshlrev_b32 v166, v165, v166\n" \
  "v_and_b32 v165, 2, v254\n" \
  "v_cndmask_b32 v166, 0, v166, vcc\n" \
  "v_cmp_eq_u32 vcc, 0, v165\n" \
  "v_mov_b32 v240, 0\n" \
  "v_mov_b32 v241, 0\n" \
  "buffer_load_dwordx4 v2, s[52:55], s65 offen offset:3072 lds\n" \
  "v_cndmask_b32 v236, 0, v166, vcc\n" \
  "v_cndmask_b32 v237, v166, v249, vcc\n" \
  "s_lshl_b32 s62, %[w], 6\n" \
  "v_mov_b32 v238, v236\n" \
  "s_mul_i32 s63, s66, 4608\n" \
  "v_mov_b32 v239, v237\n" \
  "s_add_u32 m0, m0, 0x1000\n" \
  "s_nop 0\n" \
  "buffer_load_dwordx4 v3, s[52:55], s65 offen offset:0 lds\n" \
  "s_add_u32 s63, s63, 256\n" \
  "v_mov_b32 v242, v236\n" \
  "v_mov_b32 v243, v237\n" \
  "v_lshl_add_u32 v246, v253, 3, v252\n" \
  "v_add_u32 v244, s62, v251\n" \
  "v_add_u32 v246, s63, v246\n" \
  "s_mul_i32 s65, s56, 5120\n" \
  "s_add_u32 m0, s65, s57\n" \
  "s_mul_i32 s65, s56, 4608\n" \
  "s_add_u32 s56, s56, 1\n" \
  "s_cmp_eq_u32 s56, 9\n" \
  "s_cselect_b32 s56, 0, s56\n" \
  "buffer_load_dwordx4 v0, s[52:55], s65 offen offset:0 lds\n" \
  "s_lshl_b32 s62, %[w], 7\n" \
  "v_add_u32 v245, 4608, v244\n" \
  "s_add_u32 s62, s62, 160576\n" \
  "v_lshl_add_u32 v247, v253, 4, s62\n" \
  "v_mul_u32_u24 v235, 64, v254\n" \
  "s_xor_b32 s62, %[wg], 1\n" \
  "buffer_load_dwordx4 v0, s[52:55], s65 offen offset:1024 lds\n" \
  "v_add_u32 v234, 135168, v235\n" \
  "s_mul_i32 s62, s62, 64\n" \
  "v_add_u32 v235, 136192, v235\n" \
  "s_add_u32 s62, s62, 143360\n" \
  "v_cmp_lt_u32 vcc, 31, v255\n" \
  "v_mov_b32 v164, s62\n" \
  "buffer_load_dwordx4 v1, s[52:55], s65 offen offset:2048 lds\n" \
  "s_mul_i32 s63, s66, 5120\n" \
  "s_lshl_b32 s64, s67, 11\n" \
  "v_cndmask_b32 v235, v235, v164, vcc\n" \
  "s_add_u32 s63, s63, 2566144\n" \
  "s_add_u32 s62, %[tb], %[tp]\n" \
  "s_mul_i32 s62, s62, 10240\n" \
  "buffer_load_dwordx4 v2, s[52:55], s65 offen offset:3072 lds\n" \
  "s_add_u32 s70, s62, s63\n" \
  "s_cmp_lt_u32 %[tp], 125\n" \
  "s_cselect_b32 s70, s70, 2147418112\n" \
  "s_add_u32 s71, s70, 0x1000\n" \
  "s_cmp_eq_u32 s67, 0\n" \
  "s_cselect_b32 s71, s71, 2147418112\n" \
  "s_add_u32 s70, s70, s64\n" \
  "s_add_u32 m0, m0, 0x1000\n" \
  "s_nop 0\n" \
  "buffer_load_dwordx4 v3, s[52:55], s65 offen offset:0 lds\n" \
  "s_add_u32 s62, %[tb], %[to]\n" \
  "s_mul_i32 s62, s62, 10240\n" \
  "s_add_u32 s72, s62, s63\n" \
  "s_add_u32 s73, s72, 0x1000\n" \
  "s_add_u32 s72, s72, s64\n" \
  "s_lshl_b32 s62, %[toc], 5\n" \
  "global_load_dwordx4 v[144:147], v251, s[36:37] offset:0\n" \
  "global_load_dwordx4 v[148:151], v251, s[36:37] offset:64\n" \
  "global_load_dwordx4 v[152:155], v251, s[36:37] offset:128\n" \
  "global_load_dwordx4 v[156:159], v251, s[36:37] offset:192\n" \
  "global_load_dwordx4 v[160:163], v5, s[36:37] offset:256\n" \
  "s_mul_i32 s65, s56, 5120\n" \
  "s_add_u32 m0, s65, s57\n" \
  "s_mul_i32 s65, s56, 4608\n" \
  "s_add_u32 s56, s56, 1\n" \
  "s_cmp_eq_u32 s56, 9\n" \
  "s_cselect_b32 s56, 0, s56\n" \
  "buffer_load_dwordx4 v0, s[52:55], s65 offen offset:0 lds\n" \
  "s_add_u32 s62, s62, %[bT]\n" \
  "s_mul_i32 s62, s62, 0x120\n" \
  "s_mov_b64 s[74:75], %[x]\n" \
  "s_mov_b64 s[60:61], %[out]\n" \
  "s_add_u32 s74, s74, s62\n" \
  "s_addc_u32 s75, s75, 0\n" \
  "buffer_load_dwordx4 v0, s[52:55], s65 offen offset:1024 lds\n" \
  "s_add_u32 s60, s60, s62\n" \
  "s_addc_u32 s61, s61, 0\n" \
  "s_cmp_eq_u32 %[to], 0\n" \
  "s_cselect_b32 s62, 0, 99\n" \
  "v_cmp_ne_u32 s[76:77], s62, v254\n" \
  "s_cmp_eq_u32 %[to], 124\n" \
  "s_cselect_b32 s62, 1, 99\n" \
  "buffer_load_dwordx4 v1, s[52:55], s65 offen offset:2048 lds\n" \
  "v_cmp_ne_u32 s[78:79], s62, v254\n" \
  "s_mul_i32 s58, %[w], 18432\n" \
  "s_add_u32 s80, s58, 5130240\n" \
  "s_add_u32 s58, s58, 5213184\n" \
  "s_mov_b32 s59, 5286912\n" \
  "buffer_load_dwordx4 v2, s[52:55], s65 offen offset:3072 lds\n" \
  "s_cmp_eq_u32 %[w], 3\n" \
  "s_mov_b32 s81, 2147418112\n" \
  "s_cselect_b32 s81, 5203968, s81\n" \
  "v_mov_b32 v44, 0\n" \
  "v_mov_b32 v45, 0\n" \
  "v_mov_b32 v46, 0\n" \
  "v_mov_b32 v47, 0\n" \
  "s_add_u32 m0, m0, 0x1000\n" \
  "s_nop 0\n" \
  "buffer_load_dwordx4 v3, s[52:55], s65 offen offset:0 lds\n" \
  "v_mov_b32 v48, 0\n" \
  "v_mov_b32 v49, 0\n" \
  "v_mov_b32 v50, 0\n" \
  "v_mov_b32 v51, 0\n" \
  "v_mov_b32 v52, 0\n" \
  "v_mov_b32 v53, 0\n" \
  "s_mul_i32 s65, s56, 5120\n" \
  "s_add_u32 m0, s65, s57\n" \
  "s_mul_i32 s65, s56, 4608\n" \
  "s_add_u32 s56, s56, 1\n" \
  "s_cmp_eq_u32 s56, 9\n" \
  "s_cselect_b32 s56, 0, s56\n" \
  "buffer_load_dwordx4 v0, s[52:55], s65 offen offset:0 lds\n" \
  "v_mov_b32 v54, 0\n" \
  "v_mov_b32 v55, 0\n" \
  "v_mov_b32 v56, 0\n" \
  "v_mov_b32 v57, 0\n" \
  "v_mov_b32 v58, 0\n" \
  "v_mov_b32 v59, 0\n" \
  "buffer_load_dwordx4 v0, s[52:55], s65 offen offset:1024 lds\n" \
  "v_mov_b32 v60, 0\n" \
  "v_mov_b32 v61, 0\n" \
  "v_mov_b32 v62, 0\n" \
  "v_mov_b32 v63, 0\n" \
  "v_mov_b32 v64, 0\n" \
  "v_mov_b32 v65, 0\n" \
  "buffer_load_dwordx4 v1, s[52:55], s65 offen offset:2048 lds\n" \
  "v_mov_b32 v66, 0\n" \
  "v_mov_b32 v67, 0\n" \
  "v_mov_b32 v68, 0\n" \
  "v_mov_b32 v69, 0\n" \
  "v_mov_b32 v70, 0\n" \
  "v_mov_b32 v71, 0\n" \
  "v_mov_b32 v72, 0\n" \
  "buffer_load_dwordx4 v2, s[52:55], s65 offen offset:3072 lds\n" \
  "v_mov_b32 v73, 0\n" \
  "v_mov_b32 v74, 0\n" \
  "v_mov_b32 v75, 0\n" \
  "v_mov_b32 v76, 0\n" \
  "v_mov_b32 v77, 0\n" \
  "v_mov_b32 v78, 0\n" \
  "s_add_u32 m0, m0, 0x1000\n" \
  "s_nop 0\n" \
  "buffer_load_dwordx4 v3, s[52:55], s65 offen offset:0 lds\n" \
  "v_mov_b32 v79, 0\n" \
  "v_mov_b32 v80, 0\n" \
  "v_mov_b32 v81, 0\n" \
  "v_mov_b32 v82, 0\n" \
  "v_mov_b32 v83, 0\n" \
  "v_mov_b32 v84, 0\n" \
  "s_mul_i32 s65, s56, 5120\n" \
  "s_add_u32 m0, s65, s57\n" \
  "s_mul_i32 s65, s56, 4608\n" \
  "s_add_u32 s56, s56, 1\n" \
  "s_cmp_eq_u32 s56, 9\n" \
  "s_cselect_b32 s56, 0, s56\n" \
  "buffer_load_dwordx4 v0, s[52:55], s65 offen offset:0 lds\n" \
  "v_mov_b32 v85, 0\n" \
  "v_mov_b32 v86, 0\n" \
  "v_mov_b32 v87, 0\n" \
  "v_mov_b32 v88, 0\n" \
  "v_mov_b32 v89, 0\n" \
  "v_mov_b32 v90, 0\n" \
  "buffer_load_dwordx4 v0, s[52:55], s65 offen offset:1024 lds\n" \
  "v_mov_b32 v91, 0\n" \
  "v_mov_b32 v92, 0\n" \
  "v_mov_b32 v93, 0\n" \
  "v_mov_b32 v94, 0\n" \
  "v_mov_b32 v95, 0\n" \
  "v_mov_b32 v96, 0\n" \
  "buffer_load_dwordx4 v1, s[52:55], s65 offen offset:2048 lds\n" \
  "v_mov_b32 v97, 0\n" \
  "v_mov_b32 v98, 0\n" \
  "v_mov_b32 v99, 0\n" \
  "v_mov_b32 v100, 0\n" \
  "v_mov_b32 v101, 0\n" \
  "v_mov_b32 v102, 0\n" \
  "buffer_load_dwordx4 v2, s[52:55], s65 offen offset:3072 lds\n" \
  "v_mov_b32 v103, 0\n" \
  "v_mov_b32 v104, 0\n" \
  "v_mov_b32 v105, 0\n" \
  "v_mov_b32 v106, 0\n" \
  "v_mov_b32 v107, 0\n" \
  "v_mov_b32 v108, 0\n" \
  "s_add_u32 m0, m0, 0x1000\n" \
  "s_nop 0\n" \
  "buffer_load_dwordx4 v3, s[52:55], s65 offen offset:0 lds\n" \
  "v_mov_b32 v109, 0\n" \
  "v_mov_b32 v110, 0\n" \
  "v_mov_b32 v111, 0\n" \
  "v_mov_b32 v112, 0\n" \
  "v_mov_b32 v113, 0\n" \
  "v_mov_b32 v114, 0\n" \
  "s_mul_i32 s65, s56, 5120\n" \
  "s_add_u32 m0, s65, s57\n" \
  "s_mul_i32 s65, s56, 4608\n" \
  "s_add_u32 s56, s56, 1\n" \
  "s_cmp_eq_u32 s56, 9\n" \
  "s_cselect_b32 s56, 0, s56\n" \
  "buffer_load_dwordx4 v0, s[52:55], s65 offen offset:0 lds\n" \
  "v_mov_b32 v115, 0\n" \
  "v_mov_b32 v116, 0\n" \
  "v_mov_b32 v117, 0\n" \
  "v_mov_b32 v118, 0\n" \
  "v_mov_b32 v119, 0\n" \
  "v_mov_b32 v120, 0\n" \
  "buffer_load_dwordx4 v0, s[52:55], s65 offen offset:1024 lds\n" \
  "v_mov_b32 v121, 0\n" \
  "v_mov_b32 v122, 0\n" \
  "v_mov_b32 v123, 0\n" \
  "v_mov_b32 v124, 0\n" \
  "v_mov_b32 v125, 0\n" \
  "v_mov_b32 v126, 0\n" \
  "v_mov_b32 v127, 0\n" \
  "buffer_load_dwordx4 v1, s[52:55], s65 offen offset:2048 lds\n" \
  "v_mov_b32 v128, 0\n" \
  "v_mov_b32 v129, 0\n" \
  "v_mov_b32 v130, 0\n" \
  "v_mov_b32 v131, 0\n" \
  "v_mov_b32 v132, 0\n" \
  "v_mov_b32 v133, 0\n" \
  "buffer_load_dwordx4 v2, s[52:55], s65 offen offset:3072 lds\n" \
  "v_mov_b32 v134, 0\n" \
  "v_mov_b32 v135, 0\n" \
  "v_mov_b32 v136, 0\n" \
  "v_mov_b32 v137, 0\n" \
  "v_mov_b32 v138, 0\n" \
  "v_mov_b32 v139, 0\n" \
  "s_add_u32 m0, m0, 0x1000\n" \
  "s_nop 0\n" \
  "buffer_load_dwordx4 v3, s[52:55], s65 offen offset:0 lds\n" \
  "v_mov_b32 v140, 0\n" \
  "v_mov_b32 v141, 0\n" \
  "v_mov_b32 v142, 0\n" \
  "v_mov_b32 v143, 0\n" \
  "v_mov_b32 v192, 0\n" \
  "v_mov_b32 v193, 0\n" \
  "s_mul_i32 s65, s56, 5120\n" \
  "s_add_u32 m0, s65, s57\n" \
  "s_mul_i32 s65, s56, 4608\n" \
  "s_add_u32 s56, s56, 1\n" \
  "s_cmp_eq_u32 s56, 9\n" \
  "s_cselect_b32 s56, 0, s56\n" \
  "buffer_load_dwordx4 v0, s[52:55], s65 offen offset:0 lds\n" \
  "v_mov_b32 v194, 0\n" \
  "v_mov_b32 v195, 0\n" \
  "v_mov_b32 v196, 0\n" \
  "v_mov_b32 v197, 0\n" \
  "v_mov_b32 v198, 0\n" \
  "v_mov_b32 v199, 0\n" \
  "buffer_load_dwordx4 v0, s[52:55], s65 offen offset:1024 lds\n" \
  "v_mov_b32 v200, 0\n" \
  "v_mov_b32 v201, 0\n" \
  "v_mov_b32 v202, 0\n" \
  "v_mov_b32 v203, 0\n" \
  "v_mov_b32 v204, 0\n" \
  "v_mov_b32 v205, 0\n" \
  "buffer_load_dwordx4 v1, s[52:55], s65 offen offset:2048 lds\n" \
  "v_mov_b32 v206, 0\n" \
  "v_mov_b32 v207, 0\n" \
  "v_mov_b32 v208, 0\n" \
  "v_mov_b32 v209, 0\n" \
  "v_mov_b32 v210, 0\n" \
  "v_mov_b32 v211, 0\n" \
  "buffer_load_dwordx4 v2, s[52:55], s65 offen offset:3072 lds\n" \
  "v_mov_b32 v212, 0\n" \
  "v_mov_b32 v213, 0\n" \
  "v_mov_b32 v214, 0\n" \
  "v_mov_b32 v215, 0\n" \
  "v_mov_b32 v216, 0\n" \
  "v_mov_b32 v217, 0\n" \
  "s_add_u32 m0, m0, 0x1000\n" \
  "s_nop 0\n" \
  "buffer_load_dwordx4 v3, s[52:55], s65 offen offset:0 lds\n" \
  "v_mov_b32 v218, 0\n" \
  "v_mov_b32 v219, 0\n" \
  "v_mov_b32 v220, 0\n" \
  "v_mov_b32 v221, 0\n" \
  "v_mov_b32 v222, 0\n" \
  "v_mov_b32 v223, 0\n" \
  "s_add_u32 s62, %[tb], %[to]\n" \
  "s_mul_i32 s62, s62, 10240\n" \
  "s_mul_i32 s64, s66, 5120\n" \
  "s_add_u32 s62, s62, 2566144\n" \
  "v_mov_b32 v8, -1\n" \
  "s_add_u32 s62, s62, s64\n" \
  "v_mov_b32 v9, -1\n" \
  "s_cmp_lt_u32 %[to], 125\n" \
  "v_mov_b32 v10, -1\n" \
  "s_cselect_b32 s62, s62, 2147418112\n" \
  "v_mov_b32 v11, -1\n" \
  "s_add_u32 s64, s62, 0x1000\n" \
  "buffer_store_dwordx4 v[8:11], v227, s[44:47], s62 offen offset:0 sc1\n" \
  "buffer_store_dwordx4 v[8:11], v227, s[44:47], s62 offen offset:1024 sc1\n" \
  "buffer_store_dwordx4 v[8:11], v227, s[44:47], s62 offen offset:2048 sc1\n" \
  "buffer_store_dwordx4 v[8:11], v227, s[44:47], s62 offen offset:3072 sc1\n" \
  "buffer_store_dwordx4 v[8:11], v227, s[44:47], s64 offen sc1\n" \
  "s_waitcnt vmcnt(30)\n" \
  "s_branch WX2%=\n" \
  "PUB%=:\n" \
  "s_waitcnt vmcnt(34)\n" \
  "global_load_dwordx4 v[144:147], v251, s[58:59] offset:0\n" \
  "s_waitcnt vmcnt(34)\n" \
  "global_load_dwordx4 v[148:151], v251, s[58:59] offset:64\n" \
  "s_waitcnt vmcnt(34)\n" \
  "global_load_dwordx4 v[152:155], v251, s[58:59] offset:128\n" \
  "s_waitcnt vmcnt(34)\n" \
  "global_load_dwordx4 v[156:159], v251, s[58:59] offset:192\n" \
  "s_waitcnt vmcnt(34)\n" \
  "global_load_dwordx4 v[160:163], v249, s[58:59] offset:256\n" \
  "v_mov_b32 v249, 0\n" \
  "global_load_dwordx2 v[188:189], v249, s[44:45] sc1\n" \
  "s_lshl_b32 s62, %[toc], 5\n" \
  "s_add_u32 s64, s62, 32\n" \
  "s_sub_u32 s62, s62, 1\n" \
  "s_cmp_lg_u32 %[H], 0\n" \
  "s_cselect_b32 s62, s64, s62\n" \
  "s_max_i32 s62, s62, 0\n" \
  "s_min_i32 s62, s62, 3999\n" \
  "s_add_u32 s62, s62, %[bT]\n" \
  "s_mul_i32 s62, s62, 0x120\n" \
  "s_mov_b64 s[60:61], %[x]\n" \
  "v_min_u32 v190, 35, v255\n" \
  "s_add_u32 s60, s60, s62\n" \
  "s_addc_u32 s61, s61, 0\n" \
  "v_lshlrev_b32 v190, 3, v190\n" \
  "v_min_u32 v175, 848, v227\n" \
  "global_load_dwordx2 v[186:187], v190, s[60:61]\n" \
  "global_load_dwordx4 v[40:43], v175, %[dw]\n" \
  "s_and_b32 s45, s45, 0xffff\n" \
  "s_mov_b32 s46, 5296128\n" \
  "s_mov_b32 s47, 0x20000\n" \
  "s_lshr_b32 s67, %[w], 1\n" \
  "s_mul_i32 s42, %[w], 9216\n" \
  "v_bfe_u32 v164, v255, 2, 2\n" \
  "v_bfe_u32 v165, v255, 1, 1\n" \
  "v_and_b32 v166, 1, v255\n" \
  "v_lshrrev_b32 v167, 1, v253\n" \
  "v_mul_u32_u24 v168, 0x120, v164\n" \
  "v_mul_u32_u24 v169, 2432, v250\n" \
  "v_lshl_add_u32 v168, v165, 4, v168\n" \
  "v_mul_u32_u24 v170, 5120, v167\n" \
  "v_lshl_add_u32 v168, v166, 3, v168\n" \
  "v_lshrrev_b32 v171, 3, v254\n" \
  "v_add3_u32 v181, v168, v169, 0\n" \
  "v_xor_b32 v171, v171, v167\n" \
  "v_add_u32 v181, 36864, v181\n" \
  "v_lshl_add_u32 v171, v171, 4, v252\n" \
  "v_add_u32 v180, v181, v170\n" \
  "v_lshl_add_u32 v171, v250, 3, v171\n" \
  "v_lshrrev_b32 v168, 1, v255\n" \
  "v_add_u32 v182, 82944, v171\n" \
  "s_sub_u32 s62, %[t1], 1\n" \
  "v_add_u32 v168, %[t0], v168\n" \
  "v_mul_u32_u24 v172, 12, v250\n" \
  "v_min_u32 v168, s62, v168\n" \
  "v_lshlrev_b32 v173, 2, v167\n" \
  "v_add_u32 v168, %[tb], v168\n" \
  "v_xor_b32 v170, v173, v254\n" \
  "v_lshl_add_u32 v168, v168, 1, v166\n" \
  "v_xor_b32 v170, v170, v172\n" \
  "v_mul_u32_u24 v168, 64, v168\n" \
  "v_lshlrev_b32 v170, 4, v170\n" \
  "v_add_u32 v177, 4096, v168\n" \
  "v_lshl_add_u32 v170, v250, 8, v170\n" \
  "v_add_u32 v173, v173, v164\n" \
  "v_lshl_add_u32 v170, v167, 9, v170\n" \
  "v_lshlrev_b32 v171, 2, v165\n" \
  "v_add_u32 v228, s42, v170\n" \
  "v_xor_b32 v173, v173, v171\n" \
  "v_add_u32 v224, 0, v228\n" \
  "v_xor_b32 v173, v173, v172\n" \
  "v_add_u32 v191, 129024, v227\n" \
  "v_lshlrev_b32 v173, 4, v173\n" \
  "v_lshl_add_u32 v168, v167, 5, v254\n" \
  "v_lshl_add_u32 v173, v166, 3, v173\n" \
  "v_lshlrev_b32 v168, 4, v168\n" \
  "v_lshl_add_u32 v173, v250, 8, v173\n" \
  "v_lshl_add_u32 v168, v250, 3, v168\n" \
  "v_lshl_add_u32 v173, v165, 9, v173\n" \
  "s_mul_i32 s62, s67, 9216\n" \
  "v_sub_u32 v229, v173, v170\n" \
  "s_lshl_b32 s63, s66, 8\n" \
  "v_xor_b32 v173, 0x80, v173\n" \
  "s_add_u32 s62, s62, s63\n" \
  "v_sub_u32 v230, v173, v170\n" \
  "s_add_u32 s62, s62, 129024\n" \
  "v_lshlrev_b32 v169, 2, v250\n" \
  "s_mul_i32 s63, s66, 12\n" \
  "v_xor_b32 v169, v169, v254\n" \
  "v_add_u32 v183, s62, v168\n" \
  "v_xor_b32 v169, s63, v169\n" \
  "v_mov_b32 v184, 0x3f803f80\n" \
  "v_lshlrev_b32 v169, 4, v169\n" \
  "v_mov_b32 v185, 0.006944444444444444\n" \
  "v_lshl_add_u32 v176, v253, 9, v169\n" \
  "v_add_u32 v231, 0x1000, v227\n" \
  "v_add_u32 v248, 0x2000, v227\n" \
  "v_lshrrev_b32 v164, 2, v254\n" \
  "v_and_b32 v165, 1, v254\n" \
  "v_cmp_eq_u32 vcc, v164, v253\n" \
  "v_lshlrev_b32 v165, 4, v165\n" \
  "v_mov_b32 v166, 0x3f80\n" \
  "v_lshlrev_b32 v166, v165, v166\n" \
  "v_and_b32 v165, 2, v254\n" \
  "v_cndmask_b32 v166, 0, v166, vcc\n" \
  "v_cmp_eq_u32 vcc, 0, v165\n" \
  "v_mov_b32 v240, 0\n" \
  "v_mov_b32 v241, 0\n" \
  "v_cndmask_b32 v236, 0, v166, vcc\n" \
  "v_cndmask_b32 v237, v166, v249, vcc\n" \
  "s_lshl_b32 s62, %[w], 6\n" \
  "v_mov_b32 v238, v236\n" \
  "s_mul_i32 s63, s66, 4608\n" \
  "v_mov_b32 v239, v237\n" \
  "s_add_u32 s63, s63, 256\n" \
  "v_mov_b32 v242, v236\n" \
  "v_mov_b32 v243, v237\n" \
  "v_lshl_add_u32 v246, v253, 3, v252\n" \
  "v_add_u32 v244, s62, v251\n" \
  "v_add_u32 v246, s63, v246\n" \
  "s_lshl_b32 s62, %[w], 7\n" \
  "v_add_u32 v245, 4608, v244\n" \
  "s_add_u32 s62, s62, 160576\n" \
  "v_lshl_add_u32 v247, v253, 4, s62\n" \
  "v_mul_u32_u24 v235, 64, v254\n" \
  "s_xor_b32 s62, %[wg], 1\n" \
  "v_add_u32 v234, 135168, v235\n" \
  "s_mul_i32 s62, s62, 64\n" \
  "v_add_u32 v235, 136192, v235\n" \
  "s_add_u32 s62, s62, 143360\n" \
  "v_cmp_lt_u32 vcc, 31, v255\n" \
  "v_mov_b32 v164, s62\n" \
  "s_mul_i32 s63, s66, 5120\n" \
  "s_lshl_b32 s64, s67, 11\n" \
  "v_cndmask_b32 v235, v235, v164, vcc\n" \
  "s_add_u32 s63, s63, 2566144\n" \
  "s_add_u32 s62, %[tb], %[tp]\n" \
  "s_mul_i32 s62, s62, 10240\n" \
  "s_add_u32 s70, s62, s63\n" \
  "s_cmp_lt_u32 %[tp], 125\n" \
  "s_cselect_b32 s70, s70, 2147418112\n" \
  "s_add_u32 s71, s70, 0x1000\n" \
  "s_cmp_eq_u32 s67, 0\n" \
  "s_cselect_b32 s71, s71, 2147418112\n" \
  "s_add_u32 s70, s70, s64\n" \
  "s_add_u32 s62, %[tb], %[to]\n" \
  "s_mul_i32 s62, s62, 10240\n" \
  "s_add_u32 s72, s62, s63\n" \
  "s_add_u32 s73, s72, 0x1000\n" \
  "s_add_u32 s72, s72, s64\n" \
  "s_lshl_b32 s62, %[toc], 5\n" \
  "s_add_u32 s62, s62, %[bT]\n" \
  "s_mul_i32 s62, s62, 0x120\n" \
  "s_mov_b64 s[74:75], %[x]\n" \
  "s_mov_b64 s[60:61], %[out]\n" \
  "s_add_u32 s74, s74, s62\n" \
  "s_addc_u32 s75, s75, 0\n" \
  "s_add_u32 s60, s60, s62\n" \
  "s_addc_u32 s61, s61, 0\n" \
  "s_cmp_eq_u32 %[to], 0\n" \
  "s_cselect_b32 s62, 0, 99\n" \
  "v_cmp_ne_u32 s[76:77], s62, v254\n" \
  "s_cmp_eq_u32 %[to], 124\n" \
  "s_cselect_b32 s62, 1, 99\n" \
  "v_cmp_ne_u32 s[78:79], s62, v254\n" \
  "s_mul_i32 s58, %[w], 18432\n" \
  "s_add_u32 s80, s58, 5130240\n" \
  "s_add_u32 s58, s58, 5213184\n" \
  "s_mov_b32 s59, 5286912\n" \
  "s_cmp_eq_u32 %[w], 3\n" \
  "s_mov_b32 s81, 2147418112\n" \
  "s_cselect_b32 s81, 5203968, s81\n" \
  "v_mov_b32 v44, 0\n" \
  "v_mov_b32 v45, 0\n" \
  "v_mov_b32 v46, 0\n" \
  "v_mov_b32 v47, 0\n" \
  "v_mov_b32 v48, 0\n" \
  "v_mov_b32 v49, 0\n" \
  "v_mov_b32 v50, 0\n" \
  "v_mov_b32 v51, 0\n" \
  "v_mov_b32 v52, 0\n" \
  "v_mov_b32 v53, 0\n" \
  "v_mov_b32 v54, 0\n" \
  "v_mov_b32 v55, 0\n" \
  "v_mov_b32 v56, 0\n" \
  "v_mov_b32 v57, 0\n" \
  "v_mov_b32 v58, 0\n" \
  "v_mov_b32 v59, 0\n" \
  "v_mov_b32 v60, 0\n" \
  "v_mov_b32 v61, 0\n" \
  "v_mov_b32 v62, 0\n" \
  "v_mov_b32 v63, 0\n" \
  "v_mov_b32 v64, 0\n" \
  "v_mov_b32 v65, 0\n" \
  "v_mov_b32 v66, 0\n" \
  "v_mov_b32 v67, 0\n" \
  "v_mov_b32 v68, 0\n" \
  "v_mov_b32 v69, 0\n" \
  "v_mov_b32 v70, 0\n" \
  "v_mov_b32 v71, 0\n" \
  "v_mov_b32 v72, 0\n" \
  "v_mov_b32 v73, 0\n" \
  "v_mov_b32 v74, 0\n" \
  "v_mov_b32 v75, 0\n" \
  "v_mov_b32 v76, 0\n" \
  "v_mov_b32 v77, 0\n" \
  "v_mov_b32 v78, 0\n" \
  "v_mov_b32 v79, 0\n" \
  "v_mov_b32 v80, 0\n" \
  "v_mov_b32 v81, 0\n" \
  "v_mov_b32 v82, 0\n" \
  "v_mov_b32 v83, 0\n" \
  "v_mov_b32 v84, 0\n" \
  "v_mov_b32 v85, 0\n" \
  "v_mov_b32 v86, 0\n" \
  "v_mov_b32 v87, 0\n" \
  "v_mov_b32 v88, 0\n" \
  "v_mov_b32 v89, 0\n" \
  "v_mov_b32 v90, 0\n" \
  "v_mov_b32 v91, 0\n" \
  "v_mov_b32 v92, 0\n" \
  "v_mov_b32 v93, 0\n" \
  "v_mov_b32 v94, 0\n" \
  "v_mov_b32 v95, 0\n" \
  "v_mov_b32 v96, 0\n" \
  "v_mov_b32 v97, 0\n" \
  "v_mov_b32 v98, 0\n" \
  "v_mov_b32 v99, 0\n" \
  "v_mov_b32 v100, 0\n" \
  "v_mov_b32 v101, 0\n" \
  "v_mov_b32 v102, 0\n" \
  "v_mov_b32 v103, 0\n" \
  "v_mov_b32 v104, 0\n" \
  "v_mov_b32 v105, 0\n" \
  "v_mov_b32 v106, 0\n" \
  "v_mov_b32 v107, 0\n" \
  "v_mov_b32 v108, 0\n" \
  "v_mov_b32 v109, 0\n" \
  "v_mov_b32 v110, 0\n" \
  "v_mov_b32 v111, 0\n" \
  "v_mov_b32 v112, 0\n" \
  "v_mov_b32 v113, 0\n" \
  "v_mov_b32 v114, 0\n" \
  "v_mov_b32 v115, 0\n" \
  "v_mov_b32 v116, 0\n" \
  "v_mov_b32 v117, 0\n" \
  "v_mov_b32 v118, 0\n" \
  "v_mov_b32 v119, 0\n" \
  "v_mov_b32 v120, 0\n" \
  "v_mov_b32 v121, 0\n" \
  "v_mov_b32 v122, 0\n" \
  "v_mov_b32 v123, 0\n" \
  "v_mov_b32 v124, 0\n" \
  "v_mov_b32 v125, 0\n" \
  "v_mov_b32 v126, 0\n" \
  "v_mov_b32 v127, 0\n" \
  "v_mov_b32 v128, 0\n" \
  "v_mov_b32 v129, 0\n" \
  "v_mov_b32 v130, 0\n" \
  "v_mov_b32 v131, 0\n" \
  "v_mov_b32 v132, 0\n" \
  "v_mov_b32 v133, 0\n" \
  "v_mov_b32 v134, 0\n" \
  "v_mov_b32 v135, 0\n" \
  "v_mov_b32 v136, 0\n" \
  "v_mov_b32 v137, 0\n" \
  "v_mov_b32 v138, 0\n" \
  "v_mov_b32 v139, 0\n" \
  "v_mov_b32 v140, 0\n" \
  "v_mov_b32 v141, 0\n" \
  "v_mov_b32 v142, 0\n" \
  "v_mov_b32 v143, 0\n" \
  "v_mov_b32 v192, 0\n" \
  "v_mov_b32 v193, 0\n" \
  "v_mov_b32 v194, 0\n" \
  "v_mov_b32 v195, 0\n" \
  "v_mov_b32 v196, 0\n" \
  "v_mov_b32 v197, 0\n" \
  "v_mov_b32 v198, 0\n" \
  "v_mov_b32 v199, 0\n" \
  "v_mov_b32 v200, 0\n" \
  "v_mov_b32 v201, 0\n" \
  "v_mov_b32 v202, 0\n" \
  "v_mov_b32 v203, 0\n" \
  "v_mov_b32 v204, 0\n" \
  "v_mov_b32 v205, 0\n" \
  "v_mov_b32 v206, 0\n" \
  "v_mov_b32 v207, 0\n" \
  "v_mov_b32 v208, 0\n" \
  "v_mov_b32 v209, 0\n" \
  "v_mov_b32 v210, 0\n" \
  "v_mov_b32 v211, 0\n" \
  "v_mov_b32 v212, 0\n" \
  "v_mov_b32 v213, 0\n" \
  "v_mov_b32 v214, 0\n" \
  "v_mov_b32 v215, 0\n" \
  "v_mov_b32 v216, 0\n" \
  "v_mov_b32 v217, 0\n" \
  "v_mov_b32 v218, 0\n" \
  "v_mov_b32 v219, 0\n" \
  "v_mov_b32 v220, 0\n" \
  "v_mov_b32 v221, 0\n" \
  "v_mov_b32 v222, 0\n" \
  "v_mov_b32 v223, 0\n" \
  "s_waitcnt vmcnt(3)\n" \
  "WX2%=:\n" \
  "s_mov_b32 s68, -1\n" \
  "s_mov_b32 s69, 0\n" \
  "v_cndmask_b32 v160, 0, v160, s[68:69]\n" \
  "v_cndmask_b32 v161, 0, v161, s[68:69]\n" \
  "v_cndmask_b32 v162, 0, v162, s[68:69]\n" \
  "v_cndmask_b32 v163, 0, v163, s[68:69]\n" \
  "v_dot2_f32_bf16 v164, v144, v184, 0\n" \
  "v_dot2_f32_bf16 v166, v144, v144, 0\n" \
  "v_dot2_f32_bf16 v165, v145, v184, 0\n" \
  "v_dot2_f32_bf16 v167, v145, v145, 0\n" \
  "v_dot2_f32_bf16 v164, v146, v184, v164\n" \
  "v_dot2_f32_bf16 v166, v146, v146, v166\n" \
  "v_dot2_f32_bf16 v165, v147, v184, v165\n" \
  "v_dot2_f32_bf16 v167, v147, v147, v167\n" \
  "v_dot2_f32_bf16 v164, v148, v184, v164\n" \
  "v_dot2_f32_bf16 v166, v148, v148, v166\n" \
  "v_dot2_f32_bf16 v165, v149, v184, v165\n" \
  "v_dot2_f32_bf16 v167, v149, v149, v167\n" \
  "v_dot2_f32_bf16 v164, v150, v184, v164\n" \
  "v_dot2_f32_bf16 v166, v150, v150, v166\n" \
  "v_dot2_f32_bf16 v165, v151, v184, v165\n" \
  "v_dot2_f32_bf16 v167, v151, v151, v167\n" \
  "v_dot2_f32_bf16 v164, v152, v184, v164\n" \
  "v_dot2_f32_bf16 v166, v152, v152, v166\n" \
  "v_dot2_f32_bf16 v165, v153, v184, v165\n" \
  "v_dot2_f32_bf16 v167, v153, v153, v167\n" \
  "v_dot2_f32_bf16 v164, v154, v184, v164\n" \
  "v_dot2_f32_bf16 v166, v154, v154, v166\n" \
  "v_dot2_f32_bf16 v165, v155, v184, v165\n" \
  "v_dot2_f32_bf16 v167, v155, v155, v167\n" \
  "v_dot2_f32_bf16 v164, v156, v184, v164\n" \
  "v_dot2_f32_bf16 v166, v156, v156, v166\n" \
  "v_dot2_f32_bf16 v165, v157, v184, v165\n" \
  "v_dot2_f32_bf16 v167, v157, v157, v167\n" \
  "v_dot2_f32_bf16 v164, v158, v184, v164\n" \
  "v_dot2_f32_bf16 v166, v158, v158, v166\n" \
  "v_dot2_f32_bf16 v165, v159, v184, v165\n" \
  "v_dot2_f32_bf16 v167, v159, v159, v167\n" \
  "v_dot2_f32_bf16 v164, v160, v184, v164\n" \
  "v_dot2_f32_bf16 v166, v160, v160, v166\n" \
  "v_dot2_f32_bf16 v165, v161, v184, v165\n" \
  "v_dot2_f32_bf16 v167, v161, v161, v167\n" \
  "v_dot2_f32_bf16 v164, v162, v184, v164\n" \
  "v_dot2_f32_bf16 v166, v162, v162, v166\n" \
  "v_dot2_f32_bf16 v165, v163, v184, v165\n" \
  "v_dot2_f32_bf16 v167, v163, v163, v167\n" \
  "v_cmp_eq_u32 vcc, 1, v253\n" \
  "v_and_b32 v39, 0xffff0000, v163\n" \
  "v_lshlrev_b32 v38, 16, v163\n" \
  "v_add_f32 v164, v164, v165\n" \
  "v_add_f32 v166, v166, v167\n" \
  "v_cndmask_b32 v168, 0, v39, vcc\n" \
  "v_mov_b32 v165, v164\n" \
  "v_mov_b32 v167, v166\n" \
  "v_mov_b32 v169, v168\n" \
  "v_lshlrev_b32 v0, 16, v144\n" \
  "v_and_b32 v1, 0xffff0000, v144\n" \
  "v_permlane16_swap_b32 v164, v165\n" \
  "v_permlane16_swap_b32 v166, v167\n" \
  "v_permlane16_swap_b32 v168, v169\n" \
  "v_lshlrev_b32 v2, 16, v145\n" \
  "v_and_b32 v3, 0xffff0000, v145\n" \
  "v_add_f32 v164, v164, v165\n" \
  "v_add_f32 v166, v166, v167\n" \
  "v_add_f32 v168, v168, v169\n" \
  "v_lshlrev_b32 v4, 16, v146\n" \
  "v_and_b32 v5, 0xffff0000, v146\n" \
  "v_mov_b32 v165, v164\n" \
  "v_mov_b32 v167, v166\n" \
  "v_mov_b32 v169, v168\n" \
  "v_lshlrev_b32 v6, 16, v147\n" \
  "v_and_b32 v7, 0xffff0000, v147\n" \
  "v_permlane32_swap_b32 v164, v165\n" \
  "v_permlane32_swap_b32 v166, v167\n" \
  "v_permlane32_swap_b32 v168, v169\n" \
  "v_lshlrev_b32 v8, 16, v148\n" \
  "v_and_b32 v9, 0xffff0000, v148\n" \
  "v_add_f32 v164, v164, v165\n" \
  "v_add_f32 v166, v166, v167\n" \
  "v_add_f32 v168, v168, v169\n" \
  "v_lshlrev_b32 v10, 16, v149\n" \
  "v_and_b32 v11, 0xffff0000, v149\n" \
  "v_mul_f32 v164, 0.006944444444444444, v164\n" \
  "v_lshlrev_b32 v12, 16, v150\n" \
  "v_and_b32 v13, 0xffff0000, v150\n" \
  "v_mov_b32 v167, 1e-05\n" \
  "v_mul_f32 v165, v164, v164\n" \
  "v_lshlrev_b32 v14, 16, v151\n" \
  "v_and_b32 v15, 0xffff0000, v151\n" \
  "v_sub_f32 v168, v168, v164\n" \
  "v_fma_f32 v166, v166, v185, -v165\n" \
  "v_mul_f32 v165, 0.0009765625, v165\n" \
  "v_lshlrev_b32 v16, 16, v152\n" \
  "v_and_b32 v17, 0xffff0000, v152\n" \
  "v_mul_f32 v168, 0.09090909090909091, v168\n" \
  "v_cmp_gt_f32 s[62:63], v165, v166\n" \
  "v_lshlrev_b32 v18, 16, v153\n" \
  "v_and_b32 v19, 0xffff0000, v153\n" \
  "v_lshlrev_b32 v20, 16, v154\n" \
  "v_and_b32 v21, 0xffff0000, v154\n" \
  "v_lshlrev_b32 v22, 16, v155\n" \
  "v_and_b32 v23, 0xffff0000, v155\n" \
  "v_lshlrev_b32 v24, 16, v156\n" \
  "v_and_b32 v25, 0xffff0000, v156\n" \
  "v_lshlrev_b32 v26, 16, v157\n" \
  "v_and_b32 v27, 0xffff0000, v157\n" \
  "v_lshlrev_b32 v28, 16, v158\n" \
  "v_and_b32 v29, 0xffff0000, v158\n" \
  "v_lshlrev_b32 v30, 16, v159\n" \
  "v_and_b32 v31, 0xffff0000, v159\n" \
  "v_lshlrev_b32 v32, 16, v160\n" \
  "v_and_b32 v33, 0xffff0000, v160\n" \
  "v_lshlrev_b32 v34, 16, v161\n" \
  "v_and_b32 v35, 0xffff0000, v161\n" \
  "v_lshlrev_b32 v36, 16, v162\n" \
  "v_and_b32 v37, 0xffff0000, v162\n" \
  "s_cmp_lg_u64 s[62:63], 0\n" \
  "s_cbranch_scc1 TP%=\n" \
  "TPR%=:\n" \
  "v_add_f32 v166, v166, v167\n" \
  "v_sub_f32 v168, v168, v164\n" \
  "v_rsq_f32 v170, v166\n" \
  "s_nop 0\n" \
  "v_mul_f32 v166, -v164, v170\n" \
  "v_mul_f32 v172, v168, v170\n" \
  "v_cndmask_b32 v168, 0, v166, s[68:69]\n" \
  "s_cmp_lt_u32 %[w], 2\n" \
  "s_cbranch_scc1 NOY%=\n" \
  "s_add_u32 s62, %[tb], %[to]\n" \
  "s_mul_i32 s62, s62, 9216\n" \
  "s_lshl_b32 s65, s66, 8\n" \
  "s_add_u32 s62, s62, 262144\n" \
  "s_add_u32 s62, s62, s65\n" \
  "s_cmp_lt_u32 %[to], 125\n" \
  "s_cselect_b32 s62, s62, 2147418112\n" \
  "s_add_u32 s64, s62, 0x1000\n" \
  "s_add_u32 s65, s62, 0x2000\n" \
  "s_mov_b32 s63, 0xffff\n" \
  "v_pk_fma_f32 v[152:153], v[0:1], v[170:171], v[172:173] op_sel_hi:[1,0,0]\n" \
  "v_pk_fma_f32 v[154:155], v[2:3], v[170:171], v[172:173] op_sel_hi:[1,0,0]\n" \
  "v_pk_fma_f32 v[156:157], v[4:5], v[170:171], v[172:173] op_sel_hi:[1,0,0]\n" \
  "v_pk_fma_f32 v[158:159], v[6:7], v[170:171], v[172:173] op_sel_hi:[1,0,0]\n" \
  "v_cvt_pk_bf16_f32 v144, v152, v153\n" \
  "v_cvt_pk_bf16_f32 v145, v154, v155\n" \
  "v_cvt_pk_bf16_f32 v146, v156, v157\n" \
  "v_cvt_pk_bf16_f32 v147, v158, v159\n" \
  "buffer_store_dwordx4 v[144:147], v176, s[44:47], s62 offen offset:0 sc1\n" \
  "v_pk_fma_f32 v[152:153], v[8:9], v[170:171], v[172:173] op_sel_hi:[1,0,0]\n" \
  "v_pk_fma_f32 v[154:155], v[10:11], v[170:171], v[172:173] op_sel_hi:[1,0,0]\n" \
  "v_pk_fma_f32 v[156:157], v[12:13], v[170:171], v[172:173] op_sel_hi:[1,0,0]\n" \
  "v_pk_fma_f32 v[158:159], v[14:15], v[170:171], v[172:173] op_sel_hi:[1,0,0]\n" \
  "v_cvt_pk_bf16_f32 v148, v152, v153\n" \
  "v_cvt_pk_bf16_f32 v149, v154, v155\n" \
  "v_cvt_pk_bf16_f32 v150, v156, v157\n" \
  "v_cvt_pk_bf16_f32 v151, v158, v159\n" \
  "buffer_store_dwordx4 v[148:151], v176, s[44:47], s62 offen offset:2048 sc1\n" \
  "v_pk_fma_f32 v[152:153], v[16:17], v[170:171], v[172:173] op_sel_hi:[1,0,0]\n" \
  "v_pk_fma_f32 v[154:155], v[18:19], v[170:171], v[172:173] op_sel_hi:[1,0,0]\n" \
  "v_pk_fma_f32 v[156:157], v[20:21], v[170:171], v[172:173] op_sel_hi:[1,0,0]\n" \
  "v_pk_fma_f32 v[158:159], v[22:23], v[170:171], v[172:173] op_sel_hi:[1,0,0]\n" \
  "v_cvt_pk_bf16_f32 v144, v152, v153\n" \
  "v_cvt_pk_bf16_f32 v145, v154, v155\n" \
  "v_cvt_pk_bf16_f32 v146, v156, v157\n" \
  "v_cvt_pk_bf16_f32 v147, v158, v159\n" \
  "buffer_store_dwordx4 v[144:147], v176, s[44:47], s64 offen offset:0 sc1\n" \
  "v_pk_fma_f32 v[152:153], v[24:25], v[170:171], v[172:173] op_sel_hi:[1,0,0]\n" \
  "v_pk_fma_f32 v[154:155], v[26:27], v[170:171], v[172:173] op_sel_hi:[1,0,0]\n" \
  "v_pk_fma_f32 v[156:157], v[28:29], v[170:171], v[172:173] op_sel_hi:[1,0,0]\n" \
  "v_pk_fma_f32 v[158:159], v[30:31], v[170:171], v[172:173] op_sel_hi:[1,0,0]\n" \
  "v_cvt_pk_bf16_f32 v148, v152, v153\n" \
  "v_cvt_pk_bf16_f32 v149, v154, v155\n" \
  "v_cvt_pk_bf16_f32 v150, v156, v157\n" \
  "v_cvt_pk_bf16_f32 v151, v158, v159\n" \
  "buffer_store_dwordx4 v[148:151], v176, s[44:47], s64 offen offset:2048 sc1\n" \
  "v_pk_fma_f32 v[152:153], v[32:33], v[170:171], v[172:173] op_sel_hi:[1,0,0]\n" \
  "v_pk_fma_f32 v[154:155], v[34:35], v[170:171], v[172:173] op_sel_hi:[1,0,0]\n" \
  "v_pk_fma_f32 v[156:157], v[36:37], v[170:171], v[172:173] op_sel_hi:[1,0,0]\n" \
  "v_pk_fma_f32 v[158:159], v[38:39], v[170:171], v[172:173] op_sel_hi:[1,0,0]\n" \
  "v_cvt_pk_bf16_f32 v144, v152, v153\n" \
  "v_cvt_pk_bf16_f32 v145, v154, v155\n" \
  "v_cvt_pk_bf16_f32 v146, v156, v157\n" \
  "v_cvt_pk_bf16_f32 v147, v158, v159\n" \
  "v_mov_b32 v160, 0x3f800000\n" \
  "v_bfi_b32 v160, s63, v147, v160\n" \
  "v_cndmask_b32 v147, v147, v160, vcc\n" \
  "s_mov_b64 exec, s[68:69]\n" \
  "buffer_store_dwordx4 v[144:147], v176, s[44:47], s65 offen offset:0 sc1\n" \
  "s_mov_b64 exec, -1\n" \
  "NOY%=:\n" \
  "s_nop 1\n" \
  "v_pk_fma_f32 v[0:1], v[0:1], v[170:171], v[166:167] op_sel_hi:[1,0,0]\n" \
  "v_pk_fma_f32 v[2:3], v[2:3], v[170:171], v[166:167] op_sel_hi:[1,0,0]\n" \
  "v_pk_fma_f32 v[4:5], v[4:5], v[170:171], v[166:167] op_sel_hi:[1,0,0]\n" \
  "v_pk_fma_f32 v[6:7], v[6:7], v[170:171], v[166:167] op_sel_hi:[1,0,0]\n" \
  "v_pk_fma_f32 v[8:9], v[8:9], v[170:171], v[166:167] op_sel_hi:[1,0,0]\n" \
  "v_pk_fma_f32 v[10:11], v[10:11], v[170:171], v[166:167] op_sel_hi:[1,0,0]\n" \
  "v_pk_fma_f32 v[12:13], v[12:13], v[170:171], v[166:167] op_sel_hi:[1,0,0]\n" \
  "v_pk_fma_f32 v[14:15], v[14:15], v[170:171], v[166:167] op_sel_hi:[1,0,0]\n" \
  "v_pk_fma_f32 v[16:17], v[16:17], v[170:171], v[166:167] op_sel_hi:[1,0,0]\n" \
  "v_pk_fma_f32 v[18:19], v[18:19], v[170:171], v[166:167] op_sel_hi:[1,0,0]\n" \
  "v_pk_fma_f32 v[20:21], v[20:21], v[170:171], v[166:167] op_sel_hi:[1,0,0]\n" \
  "v_pk_fma_f32 v[22:23], v[22:23], v[170:171], v[166:167] op_sel_hi:[1,0,0]\n" \
  "v_pk_fma_f32 v[24:25], v[24:25], v[170:171], v[166:167] op_sel_hi:[1,0,0]\n" \
  "v_pk_fma_f32 v[26:27], v[26:27], v[170:171], v[166:167] op_sel_hi:[1,0,0]\n" \
  "v_pk_fma_f32 v[28:29], v[28:29], v[170:171], v[166:167] op_sel_hi:[1,0,0]\n" \
  "v_pk_fma_f32 v[30:31], v[30:31], v[170:171], v[166:167] op_sel_hi:[1,0,0]\n" \
  "v_pk_fma_f32 v[32:33], v[32:33], v[170:171], v[168:169] op_sel_hi:[1,0,0]\n" \
  "v_pk_fma_f32 v[34:35], v[34:35], v[170:171], v[168:169] op_sel_hi:[1,0,0]\n" \
  "v_pk_fma_f32 v[36:37], v[36:37], v[170:171], v[168:169] op_sel_hi:[1,0,0]\n" \
  "v_pk_fma_f32 v[38:39], v[38:39], v[170:171], v[168:169] op_sel_hi:[1,0,0]\n" \
  "v_cvt_pk_bf16_f32 v144, v0, v1\n" \
  "v_cvt_pk_bf16_f32 v145, v2, v3\n" \
  "v_cvt_pk_bf16_f32 v146, v4, v5\n" \
  "v_cvt_pk_bf16_f32 v147, v6, v7\n" \
  "v_cvt_pk_bf16_f32 v148, v8, v9\n" \
  "v_cvt_pk_bf16_f32 v149, v10, v11\n" \
  "v_cvt_pk_bf16_f32 v150, v12, v13\n" \
  "v_cvt_pk_bf16_f32 v151, v14, v15\n" \
  "v_cvt_pk_bf16_f32 v152, v16, v17\n" \
  "v_cvt_pk_bf16_f32 v153, v18, v19\n" \
  "v_cvt_pk_bf16_f32 v154, v20, v21\n" \
  "v_cvt_pk_bf16_f32 v155, v22, v23\n" \
  "v_cvt_pk_bf16_f32 v156, v24, v25\n" \
  "v_cvt_pk_bf16_f32 v157, v26, v27\n" \
  "v_cvt_pk_bf16_f32 v158, v28, v29\n" \
  "v_cvt_pk_bf16_f32 v159, v30, v31\n" \
  "v_cvt_pk_bf16_f32 v160, v32, v33\n" \
  "v_cvt_pk_bf16_f32 v161, v34, v35\n" \
  "v_cvt_pk_bf16_f32 v162, v36, v37\n" \
  "v_cvt_pk_bf16_f32 v163, v38, v39\n" \
  "s_mul_i32 s62, s66, 4608\n" \
  "s_add_u32 s62, s62, 147744\n" \
  "s_cmp_lg_u32 %[H], 0\n" \
  "s_mov_b32 s64, 156960\n" \
  "s_cselect_b32 s64, 143136, s64\n" \
  "s_cselect_b32 s65, 15, 0\n" \
  "s_cmp_eq_u32 s66, %[H]\n" \
  "s_cselect_b32 s65, s65, 99\n" \
  "s_cmp_ge_u32 %[w], 2\n" \
  "s_cselect_b32 s62, s62, s64\n" \
  "v_cmp_eq_u32 vcc, s65, v254\n" \
  "v_add_u32 v176, s62, v251\n" \
  "s_cselect_b64 s[64:65], -1, vcc\n" \
  "s_mov_b64 exec, s[64:65]\n" \
  "ds_write_b128 v176, v[144:147] offset:0\n" \
  "ds_write_b128 v176, v[148:151] offset:64\n" \
  "ds_write_b128 v176, v[152:155] offset:128\n" \
  "ds_write_b128 v176, v[156:159] offset:192\n" \
  "s_and_b64 exec, s[64:65], s[68:69]\n" \
  "ds_write_b128 v176, v[160:163] offset:256\n" \
  "s_mov_b64 exec, -1\n" \
  "s_cmp_ge_u32 %[w], 2\n" \
  "s_cbranch_scc1 WB1%=\n" \
  "s_waitcnt vmcnt(5)\n" \
  "s_branch WB2%=\n" \
  "WB1%=:\n" \
  "s_waitcnt vmcnt(5)\n" \
  "v_readfirstlane_b32 s62, v188\n" \
  "v_readfirstlane_b32 s63, v189\n" \
  "s_mov_b64 s[36:37], %[ws]\n" \
  "s_mul_i32 s43, %[tb], 9216\n" \
  "s_xor_b32 s64, s62, 0x4C957F2D\n" \
  "s_xor_b32 s65, s63, 0x5851F42D\n" \
  "s_mul_i32 s48, s64, 0x7F4A7C15\n" \
  "s_mul_hi_u32 s49, s64, 0x7F4A7C15\n" \
  "s_mul_i32 s64, s64, 0x9E3779B9\n" \
  "s_mul_i32 s65, s65, 0x7F4A7C15\n" \
  "s_add_u32 s49, s49, s64\n" \
  "s_add_u32 s49, s49, s65\n" \
  "s_add_u32 s48, s48, 0xF767814F\n" \
  "s_addc_u32 s49, s49, 0x14057B7E\n" \
  "s_not_b64 s[64:65], s[62:63]\n" \
  "s_cmp_eq_u64 s[48:49], s[62:63]\n" \
  "s_cselect_b64 s[48:49], s[64:65], s[48:49]\n" \
  "s_add_u32 s43, s43, 262144\n" \
  "s_add_u32 s36, s36, s43\n" \
  "s_addc_u32 s37, s37, 0\n" \
  "s_and_b32 s37, s37, 0xffff\n" \
  "s_mul_i32 s38, %[t1], 9216\n" \
  "s_mov_b32 s39, 0x20000\n" \
  "s_mul_i32 s43, %[t0], 9216\n" \
  "s_mov_b32 s57, s43\n" \
  "s_mov_b32 s54, s38\n" \
  "s_mov_b32 s40, 0\n" \
  "s_add_u32 m0, s42, 0\n" \
  "s_add_u32 s55, s42, 73728\n" \
  "s_mov_b32 s56, 0\n" \
  "s_cmp_eq_u32 %[w], 3\n" \
  "s_cbranch_scc1 HAL%=\n" \
  "v_lshlrev_b32 v0, 16, v40\n" \
  "v_and_b32 v1, 0xffff0000, v40\n" \
  "v_lshlrev_b32 v2, 16, v41\n" \
  "v_and_b32 v3, 0xffff0000, v41\n" \
  "v_lshlrev_b32 v4, 16, v42\n" \
  "v_and_b32 v5, 0xffff0000, v42\n" \
  "v_lshlrev_b32 v6, 16, v43\n" \
  "v_and_b32 v7, 0xffff0000, v43\n" \
  "v_lshlrev_b32 v175, 1, v175\n" \
  "v_mov_b32 v8, 0\n" \
  "v_mov_b32 v9, 0\n" \
  "v_add_u32 v175, 160576, v175\n" \
  "v_mov_b32 v10, 0\n" \
  "v_mov_b32 v11, 0\n" \
  "v_mov_b32 v12, 157264\n" \
  "ds_write_b128 v175, v[0:3]\n" \
  "ds_write_b128 v175, v[4:7] offset:16\n" \
  "ds_write_b128 v12, v[8:11]\n" \
  "s_branch FLG%=\n" \
  "HAL%=:\n" \
  "v_cmp_gt_u32 vcc, 36, v255\n" \
  "s_cmp_lg_u32 %[H], 0\n" \
  "s_mov_b32 s62, 147456\n" \
  "s_cselect_b32 s62, 156960, s62\n" \
  "v_cndmask_b32 v186, 0, v186, vcc\n" \
  "v_cndmask_b32 v187, 0, v187, vcc\n" \
  "v_lshlrev_b32 v0, 16, v186\n" \
  "v_and_b32 v1, 0xffff0000, v186\n" \
  "v_lshlrev_b32 v2, 16, v187\n" \
  "v_and_b32 v3, 0xffff0000, v187\n" \
  "v_add_u32 v190, s62, v190\n" \
  "v_pk_add_f32 v[4:5], v[0:1], v[2:3]\n" \
  "v_add_f32 v4, v4, v5\n" \
  "s_nop 1\n" \
  "v_add_f32_dpp v4, v4, v4 quad_perm:[1,0,3,2] row_mask:0xf bank_mask:0xf\n" \
  "s_nop 1\n" \
  "v_add_f32_dpp v4, v4, v4 quad_perm:[2,3,0,1] row_mask:0xf bank_mask:0xf\n" \
  "s_nop 1\n" \
  "v_add_f32_dpp v4, v4, v4 row_half_mirror row_mask:0xf bank_mask:0xf\n" \
  "s_nop 1\n" \
  "v_add_f32_dpp v4, v4, v4 row_mirror row_mask:0xf bank_mask:0xf\n" \
  "v_mov_b32 v5, v4\n" \
  "s_nop 1\n" \
  "v_permlane16_swap_b32 v4, v5\n" \
  "v_add_f32 v4, v4, v5\n" \
  "v_mov_b32 v5, v4\n" \
  "s_nop 1\n" \
  "v_permlane32_swap_b32 v4, v5\n" \
  "v_add_f32 v4, v4, v5\n" \
  "v_mul_f32 v4, v185, v4\n" \
  "v_cndmask_b32 v4, 0, v4, vcc\n" \
  "v_pk_add_f32 v[0:1], v[0:1], v[4:5] op_sel_hi:[1,0] neg_lo:[0,1] neg_hi:[0,1]\n" \
  "v_pk_add_f32 v[2:3], v[2:3], v[4:5] op_sel_hi:[1,0] neg_lo:[0,1] neg_hi:[0,1]\n" \
  "v_pk_mul_f32 v[6:7], v[0:1], v[0:1]\n" \
  "v_pk_fma_f32 v[6:7], v[2:3], v[2:3], v[6:7]\n" \
  "v_add_f32 v6, v6, v7\n" \
  "s_nop 1\n" \
  "v_add_f32_dpp v6, v6, v6 quad_perm:[1,0,3,2] row_mask:0xf bank_mask:0xf\n" \
  "s_nop 1\n" \
  "v_add_f32_dpp v6, v6, v6 quad_perm:[2,3,0,1] row_mask:0xf bank_mask:0xf\n" \
  "s_nop 1\n" \
  "v_add_f32_dpp v6, v6, v6 row_half_mirror row_mask:0xf bank_mask:0xf\n" \
  "s_nop 1\n" \
  "v_add_f32_dpp v6, v6, v6 row_mirror row_mask:0xf bank_mask:0xf\n" \
  "v_mov_b32 v7, v6\n" \
  "s_nop 1\n" \
  "v_permlane16_swap_b32 v6, v7\n" \
  "v_add_f32 v6, v6, v7\n" \
  "v_mov_b32 v7, v6\n" \
  "s_nop 1\n" \
  "v_permlane32_swap_b32 v6, v7\n" \
  "v_add_f32 v6, v6, v7\n" \
  "v_mov_b32 v7, 1e-05\n" \
  "v_fmac_f32 v7, v185, v6\n" \
  "v_rsq_f32 v6, v7\n" \
  "s_nop 0\n" \
  "v_pk_mul_f32 v[0:1], v[0:1], v[6:7] op_sel_hi:[1,0]\n" \
  "v_pk_mul_f32 v[2:3], v[2:3], v[6:7] op_sel_hi:[1,0]\n" \
  "v_cvt_pk_bf16_f32 v0, v0, v1\n" \
  "v_cvt_pk_bf16_f32 v1, v2, v3\n" \
  "s_mov_b64 exec, vcc\n" \
  "ds_write_b64 v190, v[0:1]\n" \
  "s_mov_b64 exec, -1\n" \
  "FLG%=:\n" \
  "s_add_u32 s62, %[tb], %[to]\n" \
  "s_lshl_b32 s62, s62, 1\n" \
  "s_add_u32 s62, s62, s66\n" \
  "s_mul_i32 s62, s62, 64\n" \
  "s_add_u32 s62, s62, 4096\n" \
  "s_cmp_lt_u32 %[to], 125\n" \
  "s_cselect_b32 s62, s62, 2147418112\n" \
  "s_waitcnt vmcnt(0)\n" \
  "s_mov_b64 exec, 1\n" \
  "v_mov_b32 v178, s48\n" \
  "v_mov_b32 v179, s49\n" \
  "buffer_store_dwordx2 v[178:179], off, s[44:47], s62 sc1\n" \
  "s_mov_b64 exec, -1\n" \
  "WB2%=:\n" \
  "s_barrier\n" \
  "ds_read_b64_tr_b16 a[0:1], v180 offset:0\n" \
  "ds_read_b64_tr_b16 a[2:3], v180 offset:1152\n" \
  "ds_read_b64_tr_b16 a[4:5], v180 offset:32\n" \
  "ds_read_b64_tr_b16 a[6:7], v180 offset:1184\n" \
  "ds_read_b64_tr_b16 a[8:9], v180 offset:64\n" \
  "ds_read_b64_tr_b16 a[10:11], v180 offset:1216\n" \
  "ds_read_b64_tr_b16 a[12:13], v180 offset:96\n" \
  "ds_read_b64_tr_b16 a[14:15], v180 offset:1248\n" \
  "ds_read_b64_tr_b16 a[16:17], v180 offset:128\n" \
  "ds_read_b64_tr_b16 a[18:19], v180 offset:1280\n" \
  "ds_read_b64_tr_b16 a[20:21], v180 offset:160\n" \
  "ds_read_b64_tr_b16 a[22:23], v180 offset:1312\n" \
  "ds_read_b64_tr_b16 a[24:25], v180 offset:192\n" \
  "ds_read_b64_tr_b16 a[26:27], v180 offset:1344\n" \
  "s_cmp_ge_u32 %[w], 2\n" \
  "s_cbranch_scc1 TGD%=\n" \
  "v_readfirstlane_b32 s62, v188\n" \
  "v_readfirstlane_b32 s63, v189\n" \
  "s_mov_b64 s[36:37], %[ws]\n" \
  "s_mul_i32 s43, %[tb], 9216\n" \
  "s_xor_b32 s64, s62, 0x4C957F2D\n" \
  "s_xor_b32 s65, s63, 0x5851F42D\n" \
  "s_mul_i32 s48, s64, 0x7F4A7C15\n" \
  "s_mul_hi_u32 s49, s64, 0x7F4A7C15\n" \
  "s_mul_i32 s64, s64, 0x9E3779B9\n" \
  "s_mul_i32 s65, s65, 0x7F4A7C15\n" \
  "s_add_u32 s49, s49, s64\n" \
  "s_add_u32 s49, s49, s65\n" \
  "s_add_u32 s48, s48, 0xF767814F\n" \
  "s_addc_u32 s49, s49, 0x14057B7E\n" \
  "s_not_b64 s[64:65], s[62:63]\n" \
  "s_cmp_eq_u64 s[48:49], s[62:63]\n" \
  "s_cselect_b64 s[48:49], s[64:65], s[48:49]\n" \
  "s_add_u32 s43, s43, 262144\n" \
  "s_add_u32 s36, s36, s43\n" \
  "s_addc_u32 s37, s37, 0\n" \
  "s_and_b32 s37, s37, 0xffff\n" \
  "s_mul_i32 s38, %[t1], 9216\n" \
  "s_mov_b32 s39, 0x20000\n" \
  "s_mul_i32 s43, %[t0], 9216\n" \
  "s_mov_b32 s57, s43\n" \
  "s_mov_b32 s54, s38\n" \
  "s_mov_b32 s40, 0\n" \
  "s_add_u32 m0, s42, 0\n" \
  "s_add_u32 s55, s42, 73728\n" \
  "s_mov_b32 s56, 0\n" \
  "TGD%=:\n" \
  "s_waitcnt lgkmcnt(0)\n" \
  "v_mfma_f32_16x16x32_bf16 v[0:3], a[0:3], v[144:147], 0\n" \
  "ds_read_b64_tr_b16 a[28:29], v180 offset:224\n" \
  "ds_read_b64_tr_b16 a[30:31], v180 offset:1376\n" \
  "v_mfma_f32_16x16x32_bf16 v[4:7], a[4:7], v[144:147], 0\n" \
  "ds_read_b64_tr_b16 a[32:33], v180 offset:256\n" \
  "ds_read_b64_tr_b16 a[34:35], v180 offset:1408\n" \
  "v_mfma_f32_16x16x32_bf16 v[8:11], a[8:11], v[144:147], 0\n" \
  "ds_read_b64_tr_b16 a[36:37], v180 offset:10240\n" \
  "ds_read_b64_tr_b16 a[38:39], v180 offset:11392\n" \
  "v_mfma_f32_16x16x32_bf16 v[12:15], a[12:15], v[144:147], 0\n" \
  "ds_read_b64_tr_b16 a[40:41], v180 offset:10272\n" \
  "ds_read_b64_tr_b16 a[42:43], v180 offset:11424\n" \
  "v_mfma_f32_16x16x32_bf16 v[16:19], a[16:19], v[144:147], 0\n" \
  "ds_read_b64_tr_b16 a[44:45], v180 offset:10304\n" \
  "ds_read_b64_tr_b16 a[46:47], v180 offset:11456\n" \
  "v_mfma_f32_16x16x32_bf16 v[20:23], a[20:23], v[144:147], 0\n" \
  "ds_read_b64_tr_b16 a[48:49], v180 offset:10336\n" \
  "ds_read_b64_tr_b16 a[50:51], v180 offset:11488\n" \
  "v_mfma_f32_16x16x32_bf16 v[24:27], a[24:27], v[144:147], 0\n" \
  "ds_read_b64_tr_b16 a[52:53], v180 offset:10368\n" \
  "ds_read_b64_tr_b16 a[54:55], v180 offset:11520\n" \
  "s_waitcnt lgkmcnt(6)\n" \
  "v_mfma_f32_16x16x32_bf16 v[28:31], a[28:31], v[144:147], 0\n" \
  "ds_read_b64_tr_b16 a[56:57], v180 offset:10400\n" \
  "ds_read_b64_tr_b16 a[58:59], v180 offset:11552\n" \
  "v_mfma_f32_16x16x32_bf16 v[32:35], a[32:35], v[144:147], 0\n" \
  "ds_read_b64_tr_b16 a[60:61], v180 offset:10432\n" \
  "ds_read_b64_tr_b16 a[62:63], v180 offset:11584\n" \
  "v_mfma_f32_16x16x32_bf16 v[0:3], a[36:39], v[148:151], v[0:3]\n" \
  "ds_read_b64_tr_b16 a[64:65], v180 offset:10464\n" \
  "ds_read_b64_tr_b16 a[66:67], v180 offset:11616\n" \
  "v_mfma_f32_16x16x32_bf16 v[4:7], a[40:43], v[148:151], v[4:7]\n" \
  "ds_read_b64_tr_b16 a[68:69], v180 offset:10496\n" \
  "ds_read_b64_tr_b16 a[70:71], v180 offset:11648\n" \
  "s_waitcnt lgkmcnt(6)\n" \
  "v_mfma_f32_16x16x32_bf16 v[8:11], a[44:47], v[148:151], v[8:11]\n" \
  "ds_read_b64_tr_b16 a[72:73], v180 offset:20480\n" \
  "ds_read_b64_tr_b16 a[74:75], v180 offset:21632\n" \
  "v_mfma_f32_16x16x32_bf16 v[12:15], a[48:51], v[148:151], v[12:15]\n" \
  "ds_read_b64_tr_b16 a[76:77], v180 offset:20512\n" \
  "ds_read_b64_tr_b16 a[78:79], v180 offset:21664\n" \
  "v_mfma_f32_16x16x32_bf16 v[16:19], a[52:55], v[148:151], v[16:19]\n" \
  "ds_read_b64_tr_b16 a[80:81], v180 offset:20544\n" \
  "ds_read_b64_tr_b16 a[82:83], v180 offset:21696\n" \
  "v_mfma_f32_16x16x32_bf16 v[20:23], a[56:59], v[148:151], v[20:23]\n" \
  "ds_read_b64_tr_b16 a[84:85], v180 offset:20576\n" \
  "ds_read_b64_tr_b16 a[86:87], v180 offset:21728\n" \
  "s_waitcnt lgkmcnt(6)\n" \
  "v_mfma_f32_16x16x32_bf16 v[24:27], a[60:63], v[148:151], v[24:27]\n" \
  "ds_read_b64_tr_b16 a[88:89], v180 offset:20608\n" \
  "ds_read_b64_tr_b16 a[90:91], v180 offset:21760\n" \
  "v_mfma_f32_16x16x32_bf16 v[28:31], a[64:67], v[148:151], v[28:31]\n" \
  "ds_read_b64_tr_b16 a[92:93], v180 offset:20640\n" \
  "ds_read_b64_tr_b16 a[94:95], v180 offset:21792\n" \
  "v_mfma_f32_16x16x32_bf16 v[32:35], a[68:71], v[148:151], v[32:35]\n" \
  "ds_read_b64_tr_b16 a[96:97], v180 offset:20672\n" \
  "ds_read_b64_tr_b16 a[98:99], v180 offset:21824\n" \
  "v_mfma_f32_16x16x32_bf16 v[0:3], a[72:75], v[152:155], v[0:3]\n" \
  "ds_read_b64_tr_b16 a[100:101], v180 offset:20704\n" \
  "ds_read_b64_tr_b16 a[102:103], v180 offset:21856\n" \
  "s_waitcnt lgkmcnt(6)\n" \
  "v_mfma_f32_16x16x32_bf16 v[4:7], a[76:79], v[152:155], v[4:7]\n" \
  "ds_read_b64_tr_b16 a[104:105], v180 offset:20736\n" \
  "ds_read_b64_tr_b16 a[106:107], v180 offset:21888\n" \
  "v_mfma_f32_16x16x32_bf16 v[8:11], a[80:83], v[152:155], v[8:11]\n" \
  "ds_read_b64_tr_b16 a[108:109], v180 offset:30720\n" \
  "ds_read_b64_tr_b16 a[110:111], v180 offset:31872\n" \
  "v_mfma_f32_16x16x32_bf16 v[12:15], a[84:87], v[152:155], v[12:15]\n" \
  "ds_read_b64_tr_b16 a[112:113], v180 offset:30752\n" \
  "ds_read_b64_tr_b16 a[114:115], v180 offset:31904\n" \
  "v_mfma_f32_16x16x32_bf16 v[16:19], a[88:91], v[152:155], v[16:19]\n" \
  "ds_read_b64_tr_b16 a[116:117], v180 offset:30784\n" \
  "ds_read_b64_tr_b16 a[118:119], v180 offset:31936\n" \
  "s_waitcnt lgkmcnt(6)\n" \
  "v_mfma_f32_16x16x32_bf16 v[20:23], a[92:95], v[152:155], v[20:23]\n" \
  "ds_read_b64_tr_b16 a[120:121], v180 offset:30816\n" \
  "ds_read_b64_tr_b16 a[122:123], v180 offset:31968\n" \
  "v_mfma_f32_16x16x32_bf16 v[24:27], a[96:99], v[152:155], v[24:27]\n" \
  "ds_read_b64_tr_b16 a[124:125], v180 offset:30848\n" \
  "ds_read_b64_tr_b16 a[126:127], v180 offset:32000\n" \
  "v_mfma_f32_16x16x32_bf16 v[28:31], a[100:103], v[152:155], v[28:31]\n" \
  "ds_read_b64_tr_b16 a[128:129], v180 offset:30880\n" \
  "ds_read_b64_tr_b16 a[130:131], v180 offset:32032\n" \
  "v_mfma_f32_16x16x32_bf16 v[32:35], a[104:107], v[152:155], v[32:35]\n" \
  "ds_read_b64_tr_b16 a[132:133], v180 offset:30912\n" \
  "ds_read_b64_tr_b16 a[134:135], v180 offset:32064\n" \
  "s_waitcnt lgkmcnt(6)\n" \
  "v_mfma_f32_16x16x32_bf16 v[0:3], a[108:111], v[156:159], v[0:3]\n" \
  "ds_read_b64_tr_b16 a[136:137], v180 offset:30944\n" \
  "ds_read_b64_tr_b16 a[138:139], v180 offset:32096\n" \
  "v_mfma_f32_16x16x32_bf16 v[4:7], a[112:115], v[156:159], v[4:7]\n" \
  "ds_read_b64_tr_b16 a[140:141], v180 offset:30976\n" \
  "ds_read_b64_tr_b16 a[142:143], v180 offset:32128\n" \
  "buffer_load_dwordx2 v[232:233], v177, s[44:47], 0 offen sc1\n" \
  "v_mfma_f32_16x16x32_bf16 v[8:11], a[116:119], v[156:159], v[8:11]\n" \
  "ds_read_b64_tr_b16 a[144:145], v181 offset:40960\n" \
  "ds_read_b64_tr_b16 a[146:147], v181 offset:42112\n" \
  "v_mfma_f32_16x16x32_bf16 v[12:15], a[120:123], v[156:159], v[12:15]\n" \
  "ds_read_b64_tr_b16 a[148:149], v181 offset:40992\n" \
  "ds_read_b64_tr_b16 a[150:151], v181 offset:42144\n" \
  "s_waitcnt lgkmcnt(6)\n" \
  "v_mfma_f32_16x16x32_bf16 v[16:19], a[124:127], v[156:159], v[16:19]\n" \
  "ds_read_b64_tr_b16 a[152:153], v181 offset:41024\n" \
  "ds_read_b64_tr_b16 a[154:155], v181 offset:42176\n" \
  "v_mfma_f32_16x16x32_bf16 v[20:23], a[128:131], v[156:159], v[20:23]\n" \
  "ds_read_b64_tr_b16 a[156:157], v181 offset:41056\n" \
  "ds_read_b64_tr_b16 a[158:159], v181 offset:42208\n" \
  "v_mfma_f32_16x16x32_bf16 v[24:27], a[132:135], v[156:159], v[24:27]\n" \
  "ds_read_b64_tr_b16 a[160:161], v181 offset:41088\n" \
  "ds_read_b64_tr_b16 a[162:163], v181 offset:42240\n" \
  "v_mfma_f32_16x16x32_bf16 v[28:31], a[136:139], v[156:159], v[28:31]\n" \
  "ds_read_b64_tr_b16 a[164:165], v181 offset:41120\n" \
  "ds_read_b64_tr_b16 a[166:167], v181 offset:42272\n" \
  "s_waitcnt lgkmcnt(6)\n" \
  "v_mfma_f32_16x16x32_bf16 v[32:35], a[140:143], v[156:159], v[32:35]\n" \
  "ds_read_b64_tr_b16 a[168:169], v181 offset:41152\n" \
  "ds_read_b64_tr_b16 a[170:171], v181 offset:42304\n" \
  "v_mfma_f32_16x16x32_bf16 v[0:3], a[144:147], v[160:163], v[0:3]\n" \
  "ds_read_b64_tr_b16 a[172:173], v181 offset:41184\n" \
  "ds_read_b64_tr_b16 a[174:175], v181 offset:42336\n" \
  "v_mfma_f32_16x16x32_bf16 v[4:7], a[148:151], v[160:163], v[4:7]\n" \
  "ds_read_b64_tr_b16 a[176:177], v181 offset:41216\n" \
  "ds_read_b64_tr_b16 a[178:179], v181 offset:42368\n" \
  "v_mfma_f32_16x16x32_bf16 v[8:11], a[152:155], v[160:163], v[8:11]\n" \
  "ds_read_b64 a[0:1], v182 offset:0\n" \
  "ds_read_b64 a[2:3], v182 offset:32\n" \
  "s_waitcnt lgkmcnt(6)\n" \
  "v_mfma_f32_16x16x32_bf16 v[12:15], a[156:159], v[160:163], v[12:15]\n" \
  "ds_read_b64 a[4:5], v182 offset:5120\n" \
  "ds_read_b64 a[6:7], v182 offset:5152\n" \
  "v_mfma_f32_16x16x32_bf16 v[16:19], a[160:163], v[160:163], v[16:19]\n" \
  "ds_read_b64 a[8:9], v182 offset:10240\n" \
  "ds_read_b64 a[10:11], v182 offset:10272\n" \
  "v_mfma_f32_16x16x32_bf16 v[20:23], a[164:167], v[160:163], v[20:23]\n" \
  "ds_read_b64 a[12:13], v182 offset:15360\n" \
  "ds_read_b64 a[14:15], v182 offset:15392\n" \
  "v_mfma_f32_16x16x32_bf16 v[24:27], a[168:171], v[160:163], v[24:27]\n" \
  "ds_read_b64 a[16:17], v182 offset:20480\n" \
  "ds_read_b64 a[18:19], v182 offset:20512\n" \
  "s_waitcnt lgkmcnt(6)\n" \
  "v_mfma_f32_16x16x32_bf16 v[28:31], a[172:175], v[160:163], v[28:31]\n" \
  "ds_read_b64 a[20:21], v182 offset:25600\n" \
  "ds_read_b64 a[22:23], v182 offset:25632\n" \
  "v_mfma_f32_16x16x32_bf16 v[32:35], a[176:179], v[160:163], v[32:35]\n" \
  "ds_read_b64 a[24:25], v182 offset:30720\n" \
  "ds_read_b64 a[26:27], v182 offset:30752\n" \
  "v_cvt_pk_bf16_f32 v144, v0, v1\n" \
  "v_cvt_pk_bf16_f32 v145, v2, v3\n" \
  "v_cvt_pk_bf16_f32 v146, v4, v5\n" \
  "v_cvt_pk_bf16_f32 v147, v6, v7\n" \
  "v_cvt_pk_bf16_f32 v148, v8, v9\n" \
  "v_cvt_pk_bf16_f32 v149, v10, v11\n" \
  "v_cvt_pk_bf16_f32 v150, v12, v13\n" \
  "v_cvt_pk_bf16_f32 v151, v14, v15\n" \
  "v_cvt_pk_bf16_f32 v152, v16, v17\n" \
  "v_cvt_pk_bf16_f32 v153, v18, v19\n" \
  "v_cvt_pk_bf16_f32 v154, v20, v21\n" \
  "v_cvt_pk_bf16_f32 v155, v22, v23\n" \
  "v_cvt_pk_bf16_f32 v156, v24, v25\n" \
  "v_cvt_pk_bf16_f32 v157, v26, v27\n" \
  "v_cvt_pk_bf16_f32 v158, v28, v29\n" \
  "v_cvt_pk_bf16_f32 v159, v30, v31\n" \
  "v_cvt_pk_bf16_f32 v160, v32, v33\n" \
  "v_cvt_pk_bf16_f32 v161, v34, v35\n" \
  "v_mov_b32 v162, 0\n" \
  "v_mov_b32 v163, 0\n" \
  "v_mfma_f32_16x16x32_bf16 v[0:3], a[0:3], v[144:147], 0\n" \
  "ds_read_b64 a[28:29], v182 offset:35840\n" \
  "ds_read_b64 a[30:31], v182 offset:35872\n" \
  "v_mfma_f32_16x16x32_bf16 v[4:7], a[4:7], v[144:147], 0\n" \
  "ds_read_b64 a[32:33], v182 offset:40960\n" \
  "ds_read_b64 a[34:35], v182 offset:40992\n" \
  "s_waitcnt lgkmcnt(4)\n" \
  "v_mfma_f32_16x16x32_bf16 v[8:11], a[8:11], v[144:147], 0\n" \
  "ds_read_b64 a[36:37], v182 offset:64\n" \
  "ds_read_b64 a[38:39], v182 offset:96\n" \
  "v_mfma_f32_16x16x32_bf16 v[12:15], a[12:15], v[144:147], 0\n" \
  "ds_read_b64 a[40:41], v182 offset:5184\n" \
  "ds_read_b64 a[42:43], v182 offset:5216\n" \
  "v_mfma_f32_16x16x32_bf16 v[16:19], a[16:19], v[144:147], 0\n" \
  "ds_read_b64 a[44:45], v182 offset:10304\n" \
  "ds_read_b64 a[46:47], v182 offset:10336\n" \
  "v_mfma_f32_16x16x32_bf16 v[20:23], a[20:23], v[144:147], 0\n" \
  "ds_read_b64 a[48:49], v182 offset:15424\n" \
  "ds_read_b64 a[50:51], v182 offset:15456\n" \
  "v_mfma_f32_16x16x32_bf16 v[24:27], a[24:27], v[144:147], 0\n" \
  "ds_read_b64 a[52:53], v182 offset:20544\n" \
  "ds_read_b64 a[54:55], v182 offset:20576\n" \
  "s_waitcnt lgkmcnt(6)\n" \
  "v_mfma_f32_16x16x32_bf16 v[28:31], a[28:31], v[144:147], 0\n" \
  "ds_read_b64 a[56:57], v182 offset:25664\n" \
  "ds_read_b64 a[58:59], v182 offset:25696\n" \
  "v_mfma_f32_16x16x32_bf16 v[32:35], a[32:35], v[144:147], 0\n" \
  "s_waitcnt vmcnt(0)\n" \
  "v_cmp_ne_u64 vcc, s[48:49], v[232:233]\n" \
  "s_nop 0\n" \
  "s_cmp_eq_u64 vcc, 0\n" \
  "s_cselect_b32 s41, s43, 2147418112\n" \
  "s_cselect_b32 s56, 1, 0\n" \
  "ds_read_b64 a[60:61], v182 offset:30784\n" \
  "ds_read_b64 a[62:63], v182 offset:30816\n" \
  "buffer_load_dwordx2 v[186:187], v177, s[44:47], 0 offen sc1\n" \
  "v_mfma_f32_16x16x32_bf16 v[0:3], a[36:39], v[148:151], v[0:3]\n" \
  "buffer_load_dwordx4 v227, s[36:39], s41 offen offset:0 sc1 lds\n" \
  "ds_read_b64 a[64:65], v182 offset:35904\n" \
  "ds_read_b64 a[66:67], v182 offset:35936\n" \
  "v_mfma_f32_16x16x32_bf16 v[4:7], a[40:43], v[148:151], v[4:7]\n" \
  "ds_read_b64 a[68:69], v182 offset:41024\n" \
  "ds_read_b64 a[70:71], v182 offset:41056\n" \
  "s_waitcnt lgkmcnt(6)\n" \
  "v_mfma_f32_16x16x32_bf16 v[8:11], a[44:47], v[148:151], v[8:11]\n" \
  "ds_read_b64 a[72:73], v182 offset:128\n" \
  "ds_read_b64 a[74:75], v182 offset:160\n" \
  "v_mfma_f32_16x16x32_bf16 v[12:15], a[48:51], v[148:151], v[12:15]\n" \
  "ds_read_b64 a[76:77], v182 offset:5248\n" \
  "ds_read_b64 a[78:79], v182 offset:5280\n" \
  "v_mfma_f32_16x16x32_bf16 v[16:19], a[52:55], v[148:151], v[16:19]\n" \
  "buffer_load_dwordx4 v227, s[36:39], s41 offen offset:1024 sc1 lds\n" \
  "ds_read_b64 a[80:81], v182 offset:10368\n" \
  "ds_read_b64 a[82:83], v182 offset:10400\n" \
  "v_mfma_f32_16x16x32_bf16 v[20:23], a[56:59], v[148:151], v[20:23]\n" \
  "ds_read_b64 a[84:85], v182 offset:15488\n" \
  "ds_read_b64 a[86:87], v182 offset:15520\n" \
  "s_waitcnt lgkmcnt(6)\n" \
  "v_mfma_f32_16x16x32_bf16 v[24:27], a[60:63], v[148:151], v[24:27]\n" \
  "ds_read_b64 a[88:89], v182 offset:20608\n" \
  "ds_read_b64 a[90:91], v182 offset:20640\n" \
  "v_mfma_f32_16x16x32_bf16 v[28:31], a[64:67], v[148:151], v[28:31]\n" \
  "ds_read_b64 a[92:93], v182 offset:25728\n" \
  "ds_read_b64 a[94:95], v182 offset:25760\n" \
  "v_mfma_f32_16x16x32_bf16 v[32:35], a[68:71], v[148:151], v[32:35]\n" \
  "buffer_load_dwordx4 v227, s[36:39], s41 offen offset:2048 sc1 lds\n" \
  "ds_read_b64 a[96:97], v182 offset:30848\n" \
  "ds_read_b64 a[98:99], v182 offset:30880\n" \
  "v_mfma_f32_16x16x32_bf16 v[0:3], a[72:75], v[152:155], v[0:3]\n" \
  "ds_read_b64 a[100:101], v182 offset:35968\n" \
  "ds_read_b64 a[102:103], v182 offset:36000\n" \
  "s_waitcnt lgkmcnt(6)\n" \
  "v_mfma_f32_16x16x32_bf16 v[4:7], a[76:79], v[152:155], v[4:7]\n" \
  "ds_read_b64 a[104:105], v182 offset:41088\n" \
  "ds_read_b64 a[106:107], v182 offset:41120\n" \
  "v_mfma_f32_16x16x32_bf16 v[8:11], a[80:83], v[152:155], v[8:11]\n" \
  "ds_read_b64 a[108:109], v182 offset:192\n" \
  "ds_read_b64 a[110:111], v182 offset:224\n" \
  "v_mfma_f32_16x16x32_bf16 v[12:15], a[84:87], v[152:155], v[12:15]\n" \
  "buffer_load_dwordx4 v227, s[36:39], s41 offen offset:3072 sc1 lds\n" \
  "ds_read_b64 a[112:113], v182 offset:5312\n" \
  "ds_read_b64 a[114:115], v182 offset:5344\n" \
  "v_mfma_f32_16x16x32_bf16 v[16:19], a[88:91], v[152:155], v[16:19]\n" \
  "ds_read_b64 a[116:117], v182 offset:10432\n" \
  "ds_read_b64 a[118:119], v182 offset:10464\n" \
  "s_waitcnt lgkmcnt(6)\n" \
  "v_mfma_f32_16x16x32_bf16 v[20:23], a[92:95], v[152:155], v[20:23]\n" \
  "ds_read_b64 a[120:121], v182 offset:15552\n" \
  "ds_read_b64 a[122:123], v182 offset:15584\n" \
  "v_mfma_f32_16x16x32_bf16 v[24:27], a[96:99], v[152:155], v[24:27]\n" \
  "ds_read_b64 a[124:125], v182 offset:20672\n" \
  "ds_read_b64 a[126:127], v182 offset:20704\n" \
  "v_mfma_f32_16x16x32_bf16 v[28:31], a[100:103], v[152:155], v[28:31]\n" \
  "s_add_u32 m0, m0, 0x1000\n" \
  "s_nop 0\n" \
  "buffer_load_dwordx4 v231, s[36:39], s41 offen offset:0 sc1 lds\n" \
  "ds_read_b64 a[128:129], v182 offset:25792\n" \
  "ds_read_b64 a[130:131], v182 offset:25824\n" \
  "v_mfma_f32_16x16x32_bf16 v[32:35], a[104:107], v[152:155], v[32:35]\n" \
  "ds_read_b64 a[132:133], v182 offset:30912\n" \
  "ds_read_b64 a[134:135], v182 offset:30944\n" \
  "s_waitcnt lgkmcnt(5)\n" \
  "v_mfma_f32_16x16x32_bf16 v[0:3], a[108:111], v[156:159], v[0:3]\n" \
  "ds_read_b64 a[136:137], v182 offset:36032\n" \
  "ds_read_b64 a[138:139], v182 offset:36064\n" \
  "v_mfma_f32_16x16x32_bf16 v[4:7], a[112:115], v[156:159], v[4:7]\n" \
  "ds_read_b64 a[140:141], v182 offset:41152\n" \
  "ds_read_b64 a[142:143], v182 offset:41184\n" \
  "v_mfma_f32_16x16x32_bf16 v[8:11], a[116:119], v[156:159], v[8:11]\n" \
  "buffer_load_dwordx4 v231, s[36:39], s41 offen offset:1024 sc1 lds\n" \
  "ds_read_b64 a[144:145], v182 offset:256\n" \
  "v_mfma_f32_16x16x32_bf16 v[12:15], a[120:123], v[156:159], v[12:15]\n" \
  "ds_read_b64 a[148:149], v182 offset:5376\n" \
  "s_waitcnt lgkmcnt(4)\n" \
  "v_mfma_f32_16x16x32_bf16 v[16:19], a[124:127], v[156:159], v[16:19]\n" \
  "ds_read_b64 a[152:153], v182 offset:10496\n" \
  "v_mfma_f32_16x16x32_bf16 v[20:23], a[128:131], v[156:159], v[20:23]\n" \
  "ds_read_b64 a[156:157], v182 offset:15616\n" \
  "v_mfma_f32_16x16x32_bf16 v[24:27], a[132:135], v[156:159], v[24:27]\n" \
  "buffer_load_dwordx4 v231, s[36:39], s41 offen offset:2048 sc1 lds\n" \
  "ds_read_b64 a[160:161], v182 offset:20736\n" \
  "v_mfma_f32_16x16x32_bf16 v[28:31], a[136:139], v[156:159], v[28:31]\n" \
  "ds_read_b64 a[164:165], v182 offset:25856\n" \
  "s_waitcnt lgkmcnt(3)\n" \
  "v_mfma_f32_16x16x32_bf16 v[32:35], a[140:143], v[156:159], v[32:35]\n" \
  "ds_read_b64 a[168:169], v182 offset:30976\n" \
  "v_mfma_f32_16x16x32_bf16 v[0:3], a[144:147], v[160:163], v[0:3]\n" \
  "ds_read_b64 a[172:173], v182 offset:36096\n" \
  "v_mfma_f32_16x16x32_bf16 v[4:7], a[148:151], v[160:163], v[4:7]\n" \
  "buffer_load_dwordx4 v231, s[36:39], s41 offen offset:3072 sc1 lds\n" \
  "ds_read_b64 a[176:177], v182 offset:41216\n" \
  "v_mfma_f32_16x16x32_bf16 v[8:11], a[152:155], v[160:163], v[8:11]\n" \
  "s_waitcnt lgkmcnt(2)\n" \
  "v_mfma_f32_16x16x32_bf16 v[12:15], a[156:159], v[160:163], v[12:15]\n" \
  "v_mfma_f32_16x16x32_bf16 v[16:19], a[160:163], v[160:163], v[16:19]\n" \
  "v_mfma_f32_16x16x32_bf16 v[20:23], a[164:167], v[160:163], v[20:23]\n" \
  "s_add_u32 m0, m0, 0x1000\n" \
  "s_nop 0\n" \
  "buffer_load_dwordx4 v248, s[36:39], s41 offen offset:0 sc1 lds\n" \
  "s_cmp_lg_u32 s56, 0\n" \
  "s_cselect_b32 s62, 9216, 0\n" \
  "s_cselect_b32 s63, 73728, 0\n" \
  "s_add_u32 m0, s42, s63\n" \
  "s_add_u32 s43, s43, s62\n" \
  "v_mfma_f32_16x16x32_bf16 v[24:27], a[168:171], v[160:163], v[24:27]\n" \
  "s_waitcnt lgkmcnt(0)\n" \
  "v_mfma_f32_16x16x32_bf16 v[28:31], a[172:175], v[160:163], v[28:31]\n" \
  "buffer_load_dwordx2 v[188:189], v177, s[44:47], 0 offen sc1\n" \
  "v_mfma_f32_16x16x32_bf16 v[32:35], a[176:179], v[160:163], v[32:35]\n" \
  "s_cmp_ge_u32 %[w], 2\n" \
  "s_cselect_b32 s62, %[to], %[tp]\n" \
  "s_cmp_lt_u32 s62, 125\n" \
  "s_cselect_b32 s62, 1039546447, 0\n" \
  "v_cmp_eq_u32 vcc, 3, v253\n" \
  "v_mov_b32 v148, s62\n" \
  "v_pk_add_f32 v[144:145], v[0:1], v[2:3]\n" \
  "v_pk_add_f32 v[146:147], v[4:5], v[6:7]\n" \
  "v_pk_add_f32 v[144:145], v[144:145], v[8:9]\n" \
  "v_pk_add_f32 v[146:147], v[146:147], v[10:11]\n" \
  "v_pk_add_f32 v[144:145], v[144:145], v[12:13]\n" \
  "v_pk_add_f32 v[146:147], v[146:147], v[14:15]\n" \
  "v_pk_add_f32 v[144:145], v[144:145], v[16:17]\n" \
  "v_pk_add_f32 v[146:147], v[146:147], v[18:19]\n" \
  "v_pk_add_f32 v[144:145], v[144:145], v[20:21]\n" \
  "v_pk_add_f32 v[146:147], v[146:147], v[22:23]\n" \
  "v_pk_add_f32 v[144:145], v[144:145], v[24:25]\n" \
  "v_pk_add_f32 v[146:147], v[146:147], v[26:27]\n" \
  "v_pk_add_f32 v[144:145], v[144:145], v[28:29]\n" \
  "v_pk_add_f32 v[146:147], v[146:147], v[30:31]\n" \
  "v_pk_add_f32 v[144:145], v[144:145], v[32:33]\n" \
  "v_pk_add_f32 v[146:147], v[146:147], v[34:35]\n" \
  "v_pk_add_f32 v[144:145], v[144:145], v[146:147]\n" \
  "v_cndmask_b32 v146, 0, v35, vcc\n" \
  "v_add_f32 v144, v144, v145\n" \
  "v_fmac_f32 v146, -0.08333333333333333, v144\n" \
  "v_mov_b32 v147, v146\n" \
  "v_mov_b32 v36, 0\n" \
  "v_mov_b32 v37, 0\n" \
  "v_permlane16_swap_b32 v146, v147\n" \
  "v_mov_b32 v38, 0\n" \
  "v_mov_b32 v39, 0\n" \
  "v_add_f32 v146, v146, v147\n" \
  "v_mov_b32 v40, 0\n" \
  "v_mov_b32 v41, 0\n" \
  "v_mov_b32 v147, v146\n" \
  "v_mov_b32 v42, 0\n" \
  "v_mov_b32 v43, 0\n" \
  "v_permlane32_swap_b32 v146, v147\n" \
  "v_add_f32 v146, v146, v147\n" \
  "v_mul_f32 v146, 0.09090909090909091, v146\n" \
  "v_mul_f32 v146, v148, v146\n" \
  "v_pk_fma_f32 v[0:1], v[0:1], v[148:149], v[146:147] op_sel_hi:[1,0,0]\n" \
  "v_pk_fma_f32 v[2:3], v[2:3], v[148:149], v[146:147] op_sel_hi:[1,0,0]\n" \
  "v_pk_fma_f32 v[4:5], v[4:5], v[148:149], v[146:147] op_sel_hi:[1,0,0]\n" \
  "v_pk_fma_f32 v[6:7], v[6:7], v[148:149], v[146:147] op_sel_hi:[1,0,0]\n" \
  "v_pk_fma_f32 v[8:9], v[8:9], v[148:149], v[146:147] op_sel_hi:[1,0,0]\n" \
  "v_pk_fma_f32 v[10:11], v[10:11], v[148:149], v[146:147] op_sel_hi:[1,0,0]\n" \
  "v_pk_fma_f32 v[12:13], v[12:13], v[148:149], v[146:147] op_sel_hi:[1,0,0]\n" \
  "v_pk_fma_f32 v[14:15], v[14:15], v[148:149], v[146:147] op_sel_hi:[1,0,0]\n" \
  "v_pk_fma_f32 v[16:17], v[16:17], v[148:149], v[146:147] op_sel_hi:[1,0,0]\n" \
  "v_pk_fma_f32 v[18:19], v[18:19], v[148:149], v[146:147] op_sel_hi:[1,0,0]\n" \
  "v_pk_fma_f32 v[20:21], v[20:21], v[148:149], v[146:147] op_sel_hi:[1,0,0]\n" \
  "v_pk_fma_f32 v[22:23], v[22:23], v[148:149], v[146:147] op_sel_hi:[1,0,0]\n" \
  "v_pk_fma_f32 v[24:25], v[24:25], v[148:149], v[146:147] op_sel_hi:[1,0,0]\n" \
  "v_pk_fma_f32 v[26:27], v[26:27], v[148:149], v[146:147] op_sel_hi:[1,0,0]\n" \
  "v_pk_fma_f32 v[28:29], v[28:29], v[148:149], v[146:147] op_sel_hi:[1,0,0]\n" \
  "v_pk_fma_f32 v[30:31], v[30:31], v[148:149], v[146:147] op_sel_hi:[1,0,0]\n" \
  "v_pk_fma_f32 v[32:33], v[32:33], v[148:149], v[146:147] op_sel_hi:[1,0,0]\n" \
  "v_pk_fma_f32 v[34:35], v[34:35], v[148:149], v[146:147] op_sel_hi:[1,0,0]\n" \
  "v_cndmask_b32 v35, v35, v249, vcc\n" \
  "v_cvt_pk_bf16_f32 v150, v0, v1\n" \
  "v_cvt_pk_bf16_f32 v151, v2, v3\n" \
  "v_cvt_pk_bf16_f32 v152, v4, v5\n" \
  "v_cvt_pk_bf16_f32 v153, v6, v7\n" \
  "v_cvt_pk_bf16_f32 v154, v8, v9\n" \
  "v_cvt_pk_bf16_f32 v155, v10, v11\n" \
  "v_cvt_pk_bf16_f32 v156, v12, v13\n" \
  "v_cvt_pk_bf16_f32 v157, v14, v15\n" \
  "v_cvt_pk_bf16_f32 v158, v16, v17\n" \
  "v_cvt_pk_bf16_f32 v159, v18, v19\n" \
  "v_cvt_pk_bf16_f32 v160, v20, v21\n" \
  "v_cvt_pk_bf16_f32 v161, v22, v23\n" \
  "v_cvt_pk_bf16_f32 v162, v24, v25\n" \
  "v_cvt_pk_bf16_f32 v163, v26, v27\n" \
  "v_cvt_pk_bf16_f32 v164, v28, v29\n" \
  "v_cvt_pk_bf16_f32 v165, v30, v31\n" \
  "v_cvt_pk_bf16_f32 v166, v32, v33\n" \
  "v_cvt_pk_bf16_f32 v167, v34, v35\n" \
  "ds_write_b64 v183, v[150:151] offset:0\n" \
  "ds_write_b64 v183, v[152:153] offset:1024\n" \
  "ds_write_b64 v183, v[154:155] offset:2048\n" \
  "ds_write_b64 v183, v[156:157] offset:3072\n" \
  "ds_write_b64 v183, v[158:159] offset:4096\n" \
  "ds_write_b64 v183, v[160:161] offset:5120\n" \
  "ds_write_b64 v183, v[162:163] offset:6144\n" \
  "ds_write_b64 v183, v[164:165] offset:7168\n" \
  "ds_write_b64 v183, v[166:167] offset:8192\n" \
  "v_dot2_f32_bf16 v144, v150, v150, 0\n" \
  "v_dot2_f32_bf16 v145, v151, v151, 0\n" \
  "v_dot2_f32_bf16 v144, v152, v152, v144\n" \
  "v_dot2_f32_bf16 v145, v153, v153, v145\n" \
  "v_dot2_f32_bf16 v144, v154, v154, v144\n" \
  "v_dot2_f32_bf16 v145, v155, v155, v145\n" \
  "v_dot2_f32_bf16 v144, v156, v156, v144\n" \
  "v_dot2_f32_bf16 v145, v157, v157, v145\n" \
  "v_dot2_f32_bf16 v144, v158, v158, v144\n" \
  "v_dot2_f32_bf16 v145, v159, v159, v145\n" \
  "v_dot2_f32_bf16 v144, v160, v160, v144\n" \
  "v_dot2_f32_bf16 v145, v161, v161, v145\n" \
  "v_dot2_f32_bf16 v144, v162, v162, v144\n" \
  "v_dot2_f32_bf16 v145, v163, v163, v145\n" \
  "v_dot2_f32_bf16 v144, v164, v164, v144\n" \
  "v_dot2_f32_bf16 v145, v165, v165, v145\n" \
  "v_dot2_f32_bf16 v144, v166, v166, v144\n" \
  "v_dot2_f32_bf16 v145, v167, v167, v145\n" \
  "v_mov_b32 v35, 0\n" \
  "v_mov_b32 v34, 0\n" \
  "v_mov_b32 v33, 0\n" \
  "v_add_f32 v144, v144, v145\n" \
  "v_mov_b32 v145, v144\n" \
  "v_mov_b32 v0, 0\n" \
  "v_mov_b32 v1, 0\n" \
  "v_permlane16_swap_b32 v144, v145\n" \
  "v_mov_b32 v2, 0\n" \
  "v_mov_b32 v3, 0\n" \
  "v_add_f32 v144, v144, v145\n" \
  "v_mov_b32 v4, 0\n" \
  "v_mov_b32 v5, 0\n" \
  "v_mov_b32 v145, v144\n" \
  "v_mov_b32 v6, 0\n" \
  "v_mov_b32 v7, 0\n" \
  "v_permlane32_swap_b32 v144, v145\n" \
  "v_mov_b32 v8, 0\n" \
  "v_mov_b32 v9, 0\n" \
  "v_add_f32 v144, v144, v145\n" \
  "v_mov_b32 v10, 0\n" \
  "v_mov_b32 v11, 0\n" \
  "s_lshl_b32 s63, %[w], 2\n" \
  "v_cmp_nge_f32 vcc, 61.6, v144\n" \
  "s_add_u32 s63, s63, 157248\n" \
  "v_mov_b32 v146, s63\n" \
  "s_cmp_lg_u64 vcc, 0\n" \
  "s_cselect_b32 s62, 1, 0\n" \
  "v_mov_b32 v145, s62\n" \
  "ds_write_b32 v146, v145\n" \
  "v_mov_b32 v146, 157248\n" \
  "s_cmp_lg_u32 s56, 0\n" \
  "s_cbranch_scc1 NEB%=\n" \
  "s_waitcnt vmcnt(10)\n" \
  "v_cmp_ne_u64 vcc, s[48:49], v[186:187]\n" \
  "s_nop 0\n" \
  "s_cmp_lg_u64 vcc, 0\n" \
  "s_cbranch_scc1 NEB%=\n" \
  "buffer_load_dwordx4 v227, s[36:39], s43 offen offset:0 sc1 lds\n" \
  "buffer_load_dwordx4 v227, s[36:39], s43 offen offset:1024 sc1 lds\n" \
  "buffer_load_dwordx4 v227, s[36:39], s43 offen offset:2048 sc1 lds\n" \
  "buffer_load_dwordx4 v227, s[36:39], s43 offen offset:3072 sc1 lds\n" \
  "s_add_u32 m0, m0, 0x1000\n" \
  "s_nop 0\n" \
  "buffer_load_dwordx4 v231, s[36:39], s43 offen offset:0 sc1 lds\n" \
  "buffer_load_dwordx4 v231, s[36:39], s43 offen offset:1024 sc1 lds\n" \
  "buffer_load_dwordx4 v231, s[36:39], s43 offen offset:2048 sc1 lds\n" \
  "buffer_load_dwordx4 v231, s[36:39], s43 offen offset:3072 sc1 lds\n" \
  "s_add_u32 m0, m0, 0x1000\n" \
  "s_nop 0\n" \
  "buffer_load_dwordx4 v248, s[36:39], s43 offen offset:0 sc1 lds\n" \
  "s_add_u32 s43, s43, 9216\n" \
  "s_sub_u32 m0, m0, 45056\n" \
  "s_cselect_b32 m0, s55, m0\n" \
  "s_mov_b32 s56, 2\n" \
  "NEB%=:\n" \
  "s_waitcnt lgkmcnt(0)\n" \
  "s_barrier\n" \
  "ds_read_b128 v[148:151], v146\n" \
  "s_cmp_eq_u32 s56, 1\n" \
  "s_cbranch_scc1 GOT%=\n" \
  "s_cmp_eq_u32 s56, 2\n" \
  "s_cbranch_scc1 GOT0%=\n" \
  "s_waitcnt vmcnt(0)\n" \
  "v_cmp_ne_u64 vcc, s[48:49], v[188:189]\n" \
  "s_nop 0\n" \
  "s_cmp_eq_u64 vcc, 0\n" \
  "s_cbranch_scc1 PL1D%=\n" \
  "s_mov_b32 s51, 262144\n" \
  "PL1%=:\n" \
  "buffer_load_dwordx2 v[188:189], v177, s[44:47], 0 offen sc1\n" \
  "s_waitcnt vmcnt(0)\n" \
  "v_cmp_ne_u64 vcc, s[48:49], v[188:189]\n" \
  "s_nop 0\n" \
  "s_cmp_eq_u64 vcc, 0\n" \
  "s_cbranch_scc1 PL1D%=\n" \
  "s_sub_u32 s51, s51, 1\n" \
  "s_cmp_lg_u32 s51, 0\n" \
  "s_cbranch_scc1 PL1%=\n" \
  "PL1D%=:\n" \
  "buffer_load_dwordx4 v227, s[36:39], s43 offen offset:0 sc1 lds\n" \
  "buffer_load_dwordx4 v227, s[36:39], s43 offen offset:1024 sc1 lds\n" \
  "buffer_load_dwordx4 v227, s[36:39], s43 offen offset:2048 sc1 lds\n" \
  "buffer_load_dwordx4 v227, s[36:39], s43 offen offset:3072 sc1 lds\n" \
  "s_add_u32 m0, m0, 0x1000\n" \
  "s_nop 0\n" \
  "buffer_load_dwordx4 v231, s[36:39], s43 offen offset:0 sc1 lds\n" \
  "buffer_load_dwordx4 v231, s[36:39], s43 offen offset:1024 sc1 lds\n" \
  "buffer_load_dwordx4 v231, s[36:39], s43 offen offset:2048 sc1 lds\n" \
  "buffer_load_dwordx4 v231, s[36:39], s43 offen offset:3072 sc1 lds\n" \
  "s_add_u32 m0, m0, 0x1000\n" \
  "s_nop 0\n" \
  "buffer_load_dwordx4 v248, s[36:39], s43 offen offset:0 sc1 lds\n" \
  "s_add_u32 s43, s43, 9216\n" \
  "s_sub_u32 m0, m0, 45056\n" \
  "s_cselect_b32 m0, s55, m0\n" \
  "GOT0%=:\n" \
  "s_waitcnt vmcnt(0)\n" \
  "GOT%=:\n" \
  "ds_read_b128 a[0:3], v191 offset:0\n" \
  "ds_read_b128 a[4:7], v191 offset:1024\n" \
  "ds_read_b128 a[8:11], v191 offset:2048\n" \
  "ds_read_b128 a[12:15], v191 offset:3072\n" \
  "s_waitcnt vmcnt(1)\n" \
  "ds_read_b128 a[72:75], v224 offset:0\n" \
  "ds_read_b128 a[76:79], v224 offset:1024\n" \
  "ds_read_b128 a[80:83], v224 offset:2048\n" \
  "ds_read_b128 a[84:87], v224 offset:3072\n" \
  "ds_read_b128 a[16:19], v191 offset:4096\n" \
  "ds_read_b128 a[88:91], v224 offset:4096\n" \
  "ds_read_b128 a[20:23], v191 offset:5120\n" \
  "ds_read_b128 a[92:95], v224 offset:5120\n" \
  "ds_read_b128 a[24:27], v191 offset:6144\n" \
  "ds_read_b128 a[96:99], v224 offset:6144\n" \
  "buffer_load_dwordx4 v227, s[36:39], s43 offen offset:0 sc1 lds\n" \
  "buffer_load_dwordx4 v227, s[36:39], s43 offen offset:1024 sc1 lds\n" \
  "buffer_load_dwordx4 v227, s[36:39], s43 offen offset:2048 sc1 lds\n" \
  "buffer_load_dwordx4 v227, s[36:39], s43 offen offset:3072 sc1 lds\n" \
  "s_add_u32 m0, m0, 0x1000\n" \
  "s_nop 0\n" \
  "buffer_load_dwordx4 v231, s[36:39], s43 offen offset:0 sc1 lds\n" \
  "buffer_load_dwordx4 v231, s[36:39], s43 offen offset:1024 sc1 lds\n" \
  "buffer_load_dwordx4 v231, s[36:39], s43 offen offset:2048 sc1 lds\n" \
  "buffer_load_dwordx4 v231, s[36:39], s43 offen offset:3072 sc1 lds\n" \
  "s_add_u32 m0, m0, 0x1000\n" \
  "s_nop 0\n" \
  "buffer_load_dwordx4 v248, s[36:39], s43 offen offset:0 sc1 lds\n" \
  "s_add_u32 s43, s43, 9216\n" \
  "s_sub_u32 m0, m0, 45056\n" \
  "s_cselect_b32 m0, s55, m0\n" \
  "s_waitcnt lgkmcnt(11)\n" \
  "v_or3_b32 v148, v148, v149, v150\n" \
  "v_or_b32 v148, v148, v151\n" \
  "s_mov_b32 s52, 0\n" \
  "v_readfirstlane_b32 s62, v148\n" \
  "s_nop 0\n" \
  "s_cmp_lg_u32 s62, 0\n" \
  "s_cbranch_scc1 FB%=\n" \
  "FBR%=:\n" \
  "s_waitcnt lgkmcnt(3)\n" \
  "v_mfma_f32_32x32x16_bf16 v[144:159], a[72:75], a[0:3], v[192:207]\n" \
  "buffer_load_dwordx2 v[232:233], v234, s[44:47], 0 offen sc1\n" \
  "ds_read_b128 a[28:31], v191 offset:7168\n" \
  "ds_read_b128 a[100:103], v224 offset:7168\n" \
  "v_mov_b32 v12, 0\n" \
  "v_mov_b32 v13, 0\n" \
  "v_mov_b32 v14, 0\n" \
  "v_mfma_f32_32x32x16_bf16 v[144:159], a[76:79], a[4:7], v[144:159]\n" \
  "ds_read_b128 a[32:35], v191 offset:8192\n" \
  "ds_read_b128 a[104:107], v224 offset:8192\n" \
  "buffer_load_dwordx4 v227, s[36:39], s43 offen offset:0 sc1 lds\n" \
  "v_mov_b32 v15, 0\n" \
  "v_mov_b32 v16, 0\n" \
  "v_mov_b32 v17, 0\n" \
  "v_mfma_f32_32x32x16_bf16 v[144:159], a[80:83], a[8:11], v[144:159]\n" \
  "ds_read_b128 a[36:39], v191 offset:9216\n" \
  "ds_read_b128 a[40:43], v191 offset:10240\n" \
  "v_mov_b32 v18, 0\n" \
  "v_mov_b32 v19, 0\n" \
  "v_mov_b32 v20, 0\n" \
  "v_mfma_f32_32x32x16_bf16 v[144:159], a[84:87], a[12:15], v[144:159]\n" \
  "ds_read_b128 a[44:47], v191 offset:11264\n" \
  "ds_read_b128 a[48:51], v191 offset:12288\n" \
  "buffer_load_dwordx4 v227, s[36:39], s43 offen offset:1024 sc1 lds\n" \
  "v_mov_b32 v21, 0\n" \
  "v_mov_b32 v22, 0\n" \
  "v_mov_b32 v23, 0\n" \
  "v_mfma_f32_32x32x16_bf16 v[144:159], a[88:91], a[16:19], v[144:159]\n" \
  "ds_read_b128 a[52:55], v191 offset:13312\n" \
  "ds_read_b128 a[56:59], v191 offset:14336\n" \
  "v_mov_b32 v24, 0\n" \
  "v_mov_b32 v25, 0\n" \
  "v_mov_b32 v26, 0\n" \
  "s_waitcnt lgkmcnt(6)\n" \
  "v_mfma_f32_32x32x16_bf16 v[144:159], a[92:95], a[20:23], v[144:159]\n" \
  "ds_read_b128 a[60:63], v191 offset:15360\n" \
  "ds_read_b128 a[64:67], v191 offset:16384\n" \
  "buffer_load_dwordx4 v227, s[36:39], s43 offen offset:2048 sc1 lds\n" \
  "v_add_u32 v225, v224, v229\n" \
  "v_mov_b32 v27, 0\n" \
  "v_mov_b32 v28, 0\n" \
  "v_mov_b32 v29, 0\n" \
  "v_mfma_f32_32x32x16_bf16 v[144:159], a[96:99], a[24:27], v[144:159]\n" \
  "ds_read_b128 a[68:71], v191 offset:17408\n" \
  "buffer_load_dwordx4 v227, s[36:39], s43 offen offset:3072 sc1 lds\n" \
  "v_add_u32 v226, v224, v230\n" \
  "s_add_u32 m0, m0, 0x1000\n" \
  "v_mov_b32 v30, 0\n" \
  "v_mov_b32 v31, 0\n" \
  "v_mov_b32 v32, 0\n" \
  "v_mfma_f32_32x32x16_bf16 v[144:159], a[100:103], a[28:31], v[144:159]\n" \
  "buffer_load_dwordx4 v231, s[36:39], s43 offen offset:0 sc1 lds\n" \
  "s_sub_u32 s40, s40, 36864\n" \
  "s_cselect_b32 s40, 73728, s40\n" \
  "v_mfma_f32_32x32x16_bf16 v[144:159], a[104:107], a[32:35], v[144:159]\n" \
  "buffer_load_dwordx4 v231, s[36:39], s43 offen offset:1024 sc1 lds\n" \
  "buffer_load_dwordx4 v231, s[36:39], s43 offen offset:2048 sc1 lds\n" \
  "s_mul_i32 s62, %[wg], 64\n" \
  "s_add_u32 s62, s62, 143360\n" \
  "s_cmp_eq_u32 %[w], 0\n" \
  "s_cselect_b32 s62, s62, 2147418112\n" \
  "v_mov_b32 v186, s48\n" \
  "v_mov_b32 v187, s49\n" \
  "s_waitcnt lgkmcnt(1)\n" \
  "v_mfma_f32_32x32x16_bf16 v[160:175], a[72:75], a[36:39], v[208:223]\n" \
  "ds_read_b64_tr_b16 a[108:109], v225 offset:0\n" \
  "ds_read_b64_tr_b16 a[110:111], v226 offset:0\n" \
  "ds_read_b64_tr_b16 a[112:113], v225 offset:1024\n" \
  "v_mfma_f32_32x32x16_bf16 v[160:175], a[76:79], a[40:43], v[160:175]\n" \
  "ds_read_b64_tr_b16 a[114:115], v226 offset:1024\n" \
  "ds_read_b64_tr_b16 a[116:117], v225 offset:2048\n" \
  "ds_read_b64_tr_b16 a[118:119], v226 offset:2048\n" \
  "v_exp_f32 v144, v144\n" \
  "v_exp_f32 v145, v145\n" \
  "v_mfma_f32_32x32x16_bf16 v[160:175], a[80:83], a[44:47], v[160:175]\n" \
  "ds_read_b64_tr_b16 a[120:121], v225 offset:3072\n" \
  "ds_read_b64_tr_b16 a[122:123], v226 offset:3072\n" \
  "v_exp_f32 v152, v152\n" \
  "v_exp_f32 v153, v153\n" \
  "v_mfma_f32_32x32x16_bf16 v[160:175], a[84:87], a[48:51], v[160:175]\n" \
  "ds_read_b64_tr_b16 a[124:125], v225 offset:4096\n" \
  "ds_read_b64_tr_b16 a[126:127], v226 offset:4096\n" \
  "ds_read_b64_tr_b16 a[128:129], v225 offset:5120\n" \
  "v_exp_f32 v146, v146\n" \
  "v_exp_f32 v147, v147\n" \
  "v_cvt_pk_bf16_f32 v176, v144, v145\n" \
  "v_cvt_pk_bf16_f32 v180, v152, v153\n" \
  "v_mfma_f32_32x32x16_bf16 v[160:175], a[88:91], a[52:55], v[160:175]\n" \
  "ds_read_b64_tr_b16 a[130:131], v226 offset:5120\n" \
  "ds_read_b64_tr_b16 a[132:133], v225 offset:6144\n" \
  "v_exp_f32 v154, v154\n" \
  "v_exp_f32 v155, v155\n" \
  "v_add_u32 v224, s40, v228\n" \
  "v_mfma_f32_32x32x16_bf16 v[160:175], a[92:95], a[56:59], v[160:175]\n" \
  "ds_read_b64_tr_b16 a[134:135], v226 offset:6144\n" \
  "ds_read_b64_tr_b16 a[136:137], v225 offset:7168\n" \
  "ds_read_b64_tr_b16 a[138:139], v226 offset:7168\n" \
  "v_exp_f32 v148, v148\n" \
  "v_exp_f32 v149, v149\n" \
  "v_cvt_pk_bf16_f32 v177, v146, v147\n" \
  "v_cvt_pk_bf16_f32 v181, v154, v155\n" \
  "v_permlane16_swap_b32 v176, v180\n" \
  "v_mfma_f32_32x32x16_bf16 v[160:175], a[96:99], a[60:63], v[160:175]\n" \
  "ds_read_b64_tr_b16 a[140:141], v225 offset:8192\n" \
  "ds_read_b64_tr_b16 a[142:143], v226 offset:8192\n" \
  "v_exp_f32 v156, v156\n" \
  "v_exp_f32 v157, v157\n" \
  "s_waitcnt vmcnt(7)\n" \
  "v_mfma_f32_32x32x16_bf16 v[160:175], a[100:103], a[64:67], v[160:175]\n" \
  "ds_read_b128 a[72:75], v224 offset:0\n" \
  "buffer_load_dwordx4 v231, s[36:39], s43 offen offset:3072 sc1 lds\n" \
  "v_exp_f32 v150, v150\n" \
  "v_exp_f32 v151, v151\n" \
  "v_cvt_pk_bf16_f32 v178, v148, v149\n" \
  "v_cvt_pk_bf16_f32 v182, v156, v157\n" \
  "v_permlane16_swap_b32 v177, v181\n" \
  "s_add_u32 m0, m0, 0x1000\n" \
  "s_waitcnt lgkmcnt(6)\n" \
  "v_mfma_f32_32x32x16_bf16 v[160:175], a[104:107], a[68:71], v[160:175]\n" \
  "ds_read_b128 a[76:79], v224 offset:1024\n" \
  "buffer_load_dwordx4 v248, s[36:39], s43 offen offset:0 sc1 lds\n" \
  "s_mov_b64 exec, 1\n" \
  "buffer_store_dwordx2 v[186:187], off, s[44:47], s62 sc1\n" \
  "s_mov_b64 exec, -1\n" \
  "v_exp_f32 v158, v158\n" \
  "v_exp_f32 v159, v159\n" \
  "s_add_u32 s43, s43, 9216\n" \
  "s_sub_u32 m0, m0, 45056\n" \
  "s_cselect_b32 m0, s55, m0\n" \
  "v_permlane16_swap_b32 v178, v182\n" \
  "v_cvt_pk_bf16_f32 v179, v150, v151\n" \
  "v_cvt_pk_bf16_f32 v183, v158, v159\n" \
  "s_nop 1\n" \
  "v_permlane16_swap_b32 v179, v183\n" \
  "s_nop 1\n" \
  "s_waitcnt lgkmcnt(2)\n" \
  "v_mfma_f32_16x16x32_bf16 v[0:3], a[108:111], v[176:179], v[0:3]\n" \
  "ds_read_b128 a[80:83], v224 offset:2048\n" \
  "v_mfma_f32_16x16x32_bf16 v[4:7], a[108:111], v[180:183], v[4:7]\n" \
  "ds_read_b128 a[84:87], v224 offset:3072\n" \
  "v_mfma_f32_16x16x32_bf16 v[16:19], a[112:115], v[176:179], v[16:19]\n" \
  "v_exp_f32 v160, v160\n" \
  "v_mfma_f32_16x16x32_bf16 v[20:23], a[112:115], v[180:183], v[20:23]\n" \
  "v_exp_f32 v161, v161\n" \
  "v_mfma_f32_16x16x32_bf16 v[32:35], a[116:119], v[176:179], v[32:35]\n" \
  "v_exp_f32 v162, v162\n" \
  "v_mfma_f32_16x16x32_bf16 v[36:39], a[116:119], v[180:183], v[36:39]\n" \
  "v_exp_f32 v163, v163\n" \
  "v_mfma_f32_16x16x32_bf16 v[48:51], a[120:123], v[176:179], v[48:51]\n" \
  "v_exp_f32 v164, v164\n" \
  "v_mfma_f32_16x16x32_bf16 v[52:55], a[120:123], v[180:183], v[52:55]\n" \
  "v_exp_f32 v165, v165\n" \
  "v_mfma_f32_16x16x32_bf16 v[64:67], a[124:127], v[176:179], v[64:67]\n" \
  "v_exp_f32 v166, v166\n" \
  "v_mfma_f32_16x16x32_bf16 v[68:71], a[124:127], v[180:183], v[68:71]\n" \
  "v_exp_f32 v167, v167\n" \
  "v_mfma_f32_16x16x32_bf16 v[80:83], a[128:131], v[176:179], v[80:83]\n" \
  "v_exp_f32 v168, v168\n" \
  "v_mfma_f32_16x16x32_bf16 v[84:87], a[128:131], v[180:183], v[84:87]\n" \
  "v_exp_f32 v169, v169\n" \
  "v_mfma_f32_16x16x32_bf16 v[96:99], a[132:135], v[176:179], v[96:99]\n" \
  "v_exp_f32 v170, v170\n" \
  "v_mfma_f32_16x16x32_bf16 v[100:103], a[132:135], v[180:183], v[100:103]\n" \
  "v_exp_f32 v171, v171\n" \
  "v_mfma_f32_16x16x32_bf16 v[112:115], a[136:139], v[176:179], v[112:115]\n" \
  "v_exp_f32 v172, v172\n" \
  "v_mfma_f32_16x16x32_bf16 v[116:119], a[136:139], v[180:183], v[116:119]\n" \
  "v_exp_f32 v173, v173\n" \
  "v_mfma_f32_16x16x32_bf16 v[128:131], a[140:143], v[176:179], v[128:131]\n" \
  "v_exp_f32 v174, v174\n" \
  "v_mfma_f32_16x16x32_bf16 v[132:135], a[140:143], v[180:183], v[132:135]\n" \
  "v_exp_f32 v175, v175\n" \
  "s_waitcnt lgkmcnt(0)\n" \
  "v_mfma_f32_32x32x16_bf16 v[144:159], a[72:75], a[0:3], v[192:207]\n" \
  "ds_read_b128 a[88:91], v224 offset:4096\n" \
  "v_cvt_pk_bf16_f32 v184, v160, v161\n" \
  "v_cvt_pk_bf16_f32 v188, v168, v169\n" \
  "v_cmp_ne_u64 vcc, s[48:49], v[232:233]\n" \
  "s_nop 0\n" \
  "s_cmp_eq_u64 vcc, 0\n" \
  "s_cbranch_scc1 PLWD%=\n" \
  "s_mov_b32 s51, 262144\n" \
  "PLW%=:\n" \
  "buffer_load_dwordx2 v[232:233], v234, s[44:47], 0 offen sc1\n" \
  "s_waitcnt vmcnt(0)\n" \
  "v_cmp_ne_u64 vcc, s[48:49], v[232:233]\n" \
  "s_nop 0\n" \
  "s_cmp_eq_u64 vcc, 0\n" \
  "s_cbranch_scc1 PLWD%=\n" \
  "s_sub_u32 s51, s51, 1\n" \
  "s_cmp_lg_u32 s51, 0\n" \
  "s_cbranch_scc1 PLW%=\n" \
  "PLWD%=:\n" \
  "v_mfma_f32_32x32x16_bf16 v[144:159], a[76:79], a[4:7], v[144:159]\n" \
  "ds_read_b128 a[92:95], v224 offset:5120\n" \
  "buffer_load_dwordx4 v227, s[36:39], s43 offen offset:0 sc1 lds\n" \
  "v_cvt_pk_bf16_f32 v185, v162, v163\n" \
  "v_cvt_pk_bf16_f32 v189, v170, v171\n" \
  "v_mfma_f32_32x32x16_bf16 v[144:159], a[80:83], a[8:11], v[144:159]\n" \
  "buffer_load_dwordx4 a[144:147], v227, s[44:47], s80 offen offset:0 sc1\n" \
  "ds_read_b128 a[96:99], v224 offset:6144\n" \
  "v_cvt_pk_bf16_f32 v186, v164, v165\n" \
  "v_permlane16_swap_b32 v184, v188\n" \
  "v_mfma_f32_32x32x16_bf16 v[144:159], a[84:87], a[12:15], v[144:159]\n" \
  "ds_read_b128 a[100:103], v224 offset:7168\n" \
  "buffer_load_dwordx4 v227, s[36:39], s43 offen offset:1024 sc1 lds\n" \
  "v_cvt_pk_bf16_f32 v190, v172, v173\n" \
  "v_permlane16_swap_b32 v185, v189\n" \
  "s_waitcnt lgkmcnt(2)\n" \
  "v_mfma_f32_32x32x16_bf16 v[144:159], a[88:91], a[16:19], v[144:159]\n" \
  "buffer_load_dwordx4 a[148:151], v227, s[44:47], s80 offen offset:1024 sc1\n" \
  "ds_read_b128 a[104:107], v224 offset:8192\n" \
  "v_cvt_pk_bf16_f32 v187, v166, v167\n" \
  "v_cvt_pk_bf16_f32 v191, v174, v175\n" \
  "v_mfma_f32_32x32x16_bf16 v[144:159], a[92:95], a[20:23], v[144:159]\n" \
  "buffer_load_dwordx4 v227, s[36:39], s43 offen offset:2048 sc1 lds\n" \
  "v_permlane16_swap_b32 v186, v190\n" \
  "v_add_u32 v225, v224, v229\n" \
  "s_waitcnt lgkmcnt(1)\n" \
  "v_mfma_f32_32x32x16_bf16 v[144:159], a[96:99], a[24:27], v[144:159]\n" \
  "buffer_load_dwordx4 v227, s[36:39], s43 offen offset:3072 sc1 lds\n" \
  "v_permlane16_swap_b32 v187, v191\n" \
  "v_add_u32 v226, v224, v230\n" \
  "s_add_u32 m0, m0, 0x1000\n" \
  "v_mfma_f32_32x32x16_bf16 v[144:159], a[100:103], a[28:31], v[144:159]\n" \
  "buffer_load_dwordx4 v231, s[36:39], s43 offen offset:0 sc1 lds\n" \
  "s_sub_u32 s40, s40, 36864\n" \
  "s_cselect_b32 s40, 73728, s40\n" \
  "s_waitcnt lgkmcnt(0)\n" \
  "v_mfma_f32_32x32x16_bf16 v[144:159], a[104:107], a[32:35], v[144:159]\n" \
  "buffer_load_dwordx4 v231, s[36:39], s43 offen offset:1024 sc1 lds\n" \
  "v_mfma_f32_16x16x32_bf16 v[8:11], a[108:111], v[184:187], v[8:11]\n" \
  "buffer_load_dwordx4 v231, s[36:39], s43 offen offset:2048 sc1 lds\n" \
  "v_mfma_f32_16x16x32_bf16 v[12:15], a[108:111], v[188:191], v[12:15]\n" \
  "buffer_load_dwordx4 a[152:155], v227, s[44:47], s80 offen offset:2048 sc1\n" \
  "v_mfma_f32_16x16x32_bf16 v[24:27], a[112:115], v[184:187], v[24:27]\n" \
  "v_exp_f32 v144, v144\n" \
  "v_mfma_f32_16x16x32_bf16 v[28:31], a[112:115], v[188:191], v[28:31]\n" \
  "v_exp_f32 v145, v145\n" \
  "v_mfma_f32_16x16x32_bf16 v[40:43], a[116:119], v[184:187], v[40:43]\n" \
  "v_exp_f32 v146, v146\n" \
  "v_mfma_f32_16x16x32_bf16 v[44:47], a[116:119], v[188:191], v[44:47]\n" \
  "v_exp_f32 v147, v147\n" \
  "v_mfma_f32_16x16x32_bf16 v[56:59], a[120:123], v[184:187], v[56:59]\n" \
  "v_exp_f32 v148, v148\n" \
  "v_mfma_f32_16x16x32_bf16 v[60:63], a[120:123], v[188:191], v[60:63]\n" \
  "v_exp_f32 v149, v149\n" \
  "v_mfma_f32_16x16x32_bf16 v[72:75], a[124:127], v[184:187], v[72:75]\n" \
  "v_exp_f32 v150, v150\n" \
  "v_mfma_f32_16x16x32_bf16 v[76:79], a[124:127], v[188:191], v[76:79]\n" \
  "v_exp_f32 v151, v151\n" \
  "v_mfma_f32_16x16x32_bf16 v[88:91], a[128:131], v[184:187], v[88:91]\n" \
  "v_exp_f32 v152, v152\n" \
  "v_mfma_f32_16x16x32_bf16 v[92:95], a[128:131], v[188:191], v[92:95]\n" \
  "v_exp_f32 v153, v153\n" \
  "v_mfma_f32_16x16x32_bf16 v[104:107], a[132:135], v[184:187], v[104:107]\n" \
  "v_exp_f32 v154, v154\n" \
  "v_mfma_f32_16x16x32_bf16 v[108:111], a[132:135], v[188:191], v[108:111]\n" \
  "v_exp_f32 v155, v155\n" \
  "v_mfma_f32_16x16x32_bf16 v[120:123], a[136:139], v[184:187], v[120:123]\n" \
  "v_exp_f32 v156, v156\n" \
  "v_mfma_f32_16x16x32_bf16 v[124:127], a[136:139], v[188:191], v[124:127]\n" \
  "v_exp_f32 v157, v157\n" \
  "v_mfma_f32_16x16x32_bf16 v[136:139], a[140:143], v[184:187], v[136:139]\n" \
  "v_exp_f32 v158, v158\n" \
  "v_mfma_f32_16x16x32_bf16 v[140:143], a[140:143], v[188:191], v[140:143]\n" \
  "v_exp_f32 v159, v159\n" \
  "v_mfma_f32_32x32x16_bf16 v[160:175], a[72:75], a[36:39], v[208:223]\n" \
  "ds_read_b64_tr_b16 a[108:109], v225 offset:0\n" \
  "ds_read_b64_tr_b16 a[110:111], v226 offset:0\n" \
  "ds_read_b64_tr_b16 a[112:113], v225 offset:1024\n" \
  "v_cvt_pk_bf16_f32 v176, v144, v145\n" \
  "v_cvt_pk_bf16_f32 v180, v152, v153\n" \
  "v_mfma_f32_32x32x16_bf16 v[160:175], a[76:79], a[40:43], v[160:175]\n" \
  "ds_read_b64_tr_b16 a[114:115], v226 offset:1024\n" \
  "ds_read_b64_tr_b16 a[116:117], v225 offset:2048\n" \
  "ds_read_b64_tr_b16 a[118:119], v226 offset:2048\n" \
  "v_cvt_pk_bf16_f32 v177, v146, v147\n" \
  "v_cvt_pk_bf16_f32 v181, v154, v155\n" \
  "v_mfma_f32_32x32x16_bf16 v[160:175], a[80:83], a[44:47], v[160:175]\n" \
  "buffer_load_dwordx4 a[156:159], v227, s[44:47], s80 offen offset:3072 sc1\n" \
  "ds_read_b64_tr_b16 a[120:121], v225 offset:3072\n" \
  "ds_read_b64_tr_b16 a[122:123], v226 offset:3072\n" \
  "v_cvt_pk_bf16_f32 v178, v148, v149\n" \
  "v_permlane16_swap_b32 v176, v180\n" \
  "s_add_u32 s80, s80, 0x1000\n" \
  "v_mfma_f32_32x32x16_bf16 v[160:175], a[84:87], a[48:51], v[160:175]\n" \
  "ds_read_b64_tr_b16 a[124:125], v225 offset:4096\n" \
  "ds_read_b64_tr_b16 a[126:127], v226 offset:4096\n" \
  "ds_read_b64_tr_b16 a[128:129], v225 offset:5120\n" \
  "v_cvt_pk_bf16_f32 v182, v156, v157\n" \
  "v_permlane16_swap_b32 v177, v181\n" \
  "v_mfma_f32_32x32x16_bf16 v[160:175], a[88:91], a[52:55], v[160:175]\n" \
  "ds_read_b64_tr_b16 a[130:131], v226 offset:5120\n" \
  "ds_read_b64_tr_b16 a[132:133], v225 offset:6144\n" \
  "v_cvt_pk_bf16_f32 v179, v150, v151\n" \
  "v_cvt_pk_bf16_f32 v183, v158, v159\n" \
  "v_add_u32 v224, s40, v228\n" \
  "v_mfma_f32_32x32x16_bf16 v[160:175], a[92:95], a[56:59], v[160:175]\n" \
  "buffer_load_dwordx4 a[160:163], v227, s[44:47], s80 offen offset:0 sc1\n" \
  "ds_read_b64_tr_b16 a[134:135], v226 offset:6144\n" \
  "ds_read_b64_tr_b16 a[136:137], v225 offset:7168\n" \
  "ds_read_b64_tr_b16 a[138:139], v226 offset:7168\n" \
  "v_permlane16_swap_b32 v178, v182\n" \
  "v_mfma_f32_32x32x16_bf16 v[160:175], a[96:99], a[60:63], v[160:175]\n" \
  "ds_read_b64_tr_b16 a[140:141], v225 offset:8192\n" \
  "ds_read_b64_tr_b16 a[142:143], v226 offset:8192\n" \
  "v_permlane16_swap_b32 v179, v183\n" \
  "s_waitcnt vmcnt(13)\n" \
  "v_mfma_f32_32x32x16_bf16 v[160:175], a[100:103], a[64:67], v[160:175]\n" \
  "ds_read_b128 a[72:75], v224 offset:0\n" \
  "buffer_load_dwordx4 v231, s[36:39], s43 offen offset:3072 sc1 lds\n" \
  "s_add_u32 m0, m0, 0x1000\n" \
  "v_mfma_f32_32x32x16_bf16 v[160:175], a[104:107], a[68:71], v[160:175]\n" \
  "ds_read_b128 a[76:79], v224 offset:1024\n" \
  "buffer_load_dwordx4 v248, s[36:39], s43 offen offset:0 sc1 lds\n" \
  "s_add_u32 s43, s43, 9216\n" \
  "s_sub_u32 m0, m0, 45056\n" \
  "s_cselect_b32 m0, s55, m0\n" \
  "s_waitcnt lgkmcnt(2)\n" \
  "v_mfma_f32_16x16x32_bf16 v[0:3], a[108:111], v[176:179], v[0:3]\n" \
  "ds_read_b128 a[80:83], v224 offset:2048\n" \
  "v_mfma_f32_16x16x32_bf16 v[4:7], a[108:111], v[180:183], v[4:7]\n" \
  "ds_read_b128 a[84:87], v224 offset:3072\n" \
  "v_mfma_f32_16x16x32_bf16 v[16:19], a[112:115], v[176:179], v[16:19]\n" \
  "v_exp_f32 v160, v160\n" \
  "v_mfma_f32_16x16x32_bf16 v[20:23], a[112:115], v[180:183], v[20:23]\n" \
  "v_exp_f32 v161, v161\n" \
  "v_mfma_f32_16x16x32_bf16 v[32:35], a[116:119], v[176:179], v[32:35]\n" \
  "buffer_load_dwordx4 a[164:167], v227, s[44:47], s80 offen offset:1024 sc1\n" \
  "v_exp_f32 v162, v162\n" \
  "v_mfma_f32_16x16x32_bf16 v[36:39], a[116:119], v[180:183], v[36:39]\n" \
  "v_exp_f32 v163, v163\n" \
  "v_mfma_f32_16x16x32_bf16 v[48:51], a[120:123], v[176:179], v[48:51]\n" \
  "v_exp_f32 v164, v164\n" \
  "v_mfma_f32_16x16x32_bf16 v[52:55], a[120:123], v[180:183], v[52:55]\n" \
  "v_exp_f32 v165, v165\n" \
  "v_mfma_f32_16x16x32_bf16 v[64:67], a[124:127], v[176:179], v[64:67]\n" \
  "v_exp_f32 v166, v166\n" \
  "v_mfma_f32_16x16x32_bf16 v[68:71], a[124:127], v[180:183], v[68:71]\n" \
  "v_exp_f32 v167, v167\n" \
  "v_mfma_f32_16x16x32_bf16 v[80:83], a[128:131], v[176:179], v[80:83]\n" \
  "v_exp_f32 v168, v168\n" \
  "v_mfma_f32_16x16x32_bf16 v[84:87], a[128:131], v[180:183], v[84:87]\n" \
  "v_exp_f32 v169, v169\n" \
  "v_mfma_f32_16x16x32_bf16 v[96:99], a[132:135], v[176:179], v[96:99]\n" \
  "buffer_load_dwordx4 a[168:171], v227, s[44:47], s80 offen offset:2048 sc1\n" \
  "v_exp_f32 v170, v170\n" \
  "v_mfma_f32_16x16x32_bf16 v[100:103], a[132:135], v[180:183], v[100:103]\n" \
  "v_exp_f32 v171, v171\n" \
  "v_mfma_f32_16x16x32_bf16 v[112:115], a[136:139], v[176:179], v[112:115]\n" \
  "v_exp_f32 v172, v172\n" \
  "v_mfma_f32_16x16x32_bf16 v[116:119], a[136:139], v[180:183], v[116:119]\n" \
  "v_exp_f32 v173, v173\n" \
  "v_mfma_f32_16x16x32_bf16 v[128:131], a[140:143], v[176:179], v[128:131]\n" \
  "v_exp_f32 v174, v174\n" \
  "v_mfma_f32_16x16x32_bf16 v[132:135], a[140:143], v[180:183], v[132:135]\n" \
  "v_exp_f32 v175, v175\n" \
  "s_waitcnt lgkmcnt(0)\n" \
  "v_mfma_f32_32x32x16_bf16 v[144:159], a[72:75], a[0:3], v[192:207]\n" \
  "ds_read_b128 a[88:91], v224 offset:4096\n" \
  "v_cvt_pk_bf16_f32 v184, v160, v161\n" \
  "v_cvt_pk_bf16_f32 v188, v168, v169\n" \
  "v_mfma_f32_32x32x16_bf16 v[144:159], a[76:79], a[4:7], v[144:159]\n" \
  "ds_read_b128 a[92:95], v224 offset:5120\n" \
  "buffer_load_dwordx4 v227, s[36:39], s43 offen offset:0 sc1 lds\n" \
  "v_cvt_pk_bf16_f32 v185, v162, v163\n" \
  "v_cvt_pk_bf16_f32 v189, v170, v171\n" \
  "v_mfma_f32_32x32x16_bf16 v[144:159], a[80:83], a[8:11], v[144:159]\n" \
  "buffer_load_dwordx4 a[172:175], v227, s[44:47], s80 offen offset:3072 sc1\n" \
  "ds_read_b128 a[96:99], v224 offset:6144\n" \
  "v_cvt_pk_bf16_f32 v186, v164, v165\n" \
  "v_permlane16_swap_b32 v184, v188\n" \
  "s_add_u32 s80, s80, 0x1000\n" \
  "v_mfma_f32_32x32x16_bf16 v[144:159], a[84:87], a[12:15], v[144:159]\n" \
  "ds_read_b128 a[100:103], v224 offset:7168\n" \
  "buffer_load_dwordx4 v227, s[36:39], s43 offen offset:1024 sc1 lds\n" \
  "v_cvt_pk_bf16_f32 v190, v172, v173\n" \
  "v_permlane16_swap_b32 v185, v189\n" \
  "s_waitcnt lgkmcnt(2)\n" \
  "v_mfma_f32_32x32x16_bf16 v[144:159], a[88:91], a[16:19], v[144:159]\n" \
  "buffer_load_dwordx4 a[176:179], v227, s[44:47], s80 offen offset:0 sc1\n" \
  "ds_read_b128 a[104:107], v224 offset:8192\n" \
  "v_cvt_pk_bf16_f32 v187, v166, v167\n" \
  "v_cvt_pk_bf16_f32 v191, v174, v175\n" \
  "v_mfma_f32_32x32x16_bf16 v[144:159], a[92:95], a[20:23], v[144:159]\n" \
  "buffer_load_dwordx4 v227, s[36:39], s43 offen offset:2048 sc1 lds\n" \
  "v_permlane16_swap_b32 v186, v190\n" \
  "v_add_u32 v225, v224, v229\n" \
  "s_waitcnt lgkmcnt(1)\n" \
  "v_mfma_f32_32x32x16_bf16 v[144:159], a[96:99], a[24:27], v[144:159]\n" \
  "buffer_load_dwordx4 v227, s[36:39], s43 offen offset:3072 sc1 lds\n" \
  "v_permlane16_swap_b32 v187, v191\n" \
  "v_add_u32 v226, v224, v230\n" \
  "s_add_u32 m0, m0, 0x1000\n" \
  "v_mfma_f32_32x32x16_bf16 v[144:159], a[100:103], a[28:31], v[144:159]\n" \
  "buffer_load_dwordx4 v231, s[36:39], s43 offen offset:0 sc1 lds\n" \
  "s_sub_u32 s40, s40, 36864\n" \
  "s_cselect_b32 s40, 73728, s40\n" \
  "s_waitcnt lgkmcnt(0)\n" \
  "v_mfma_f32_32x32x16_bf16 v[144:159], a[104:107], a[32:35], v[144:159]\n" \
  "buffer_load_dwordx4 v231, s[36:39], s43 offen offset:1024 sc1 lds\n" \
  "v_mfma_f32_16x16x32_bf16 v[8:11], a[108:111], v[184:187], v[8:11]\n" \
  "buffer_load_dwordx4 v231, s[36:39], s43 offen offset:2048 sc1 lds\n" \
  "v_mfma_f32_16x16x32_bf16 v[12:15], a[108:111], v[188:191], v[12:15]\n" \
  "buffer_load_dwordx4 a[180:183], v227, s[44:47], s80 offen offset:1024 sc1\n" \
  "v_mfma_f32_16x16x32_bf16 v[24:27], a[112:115], v[184:187], v[24:27]\n" \
  "v_exp_f32 v144, v144\n" \
  "v_mfma_f32_16x16x32_bf16 v[28:31], a[112:115], v[188:191], v[28:31]\n" \
  "v_exp_f32 v145, v145\n" \
  "v_mfma_f32_16x16x32_bf16 v[40:43], a[116:119], v[184:187], v[40:43]\n" \
  "v_exp_f32 v146, v146\n" \
  "v_mfma_f32_16x16x32_bf16 v[44:47], a[116:119], v[188:191], v[44:47]\n" \
  "v_exp_f32 v147, v147\n" \
  "v_mfma_f32_16x16x32_bf16 v[56:59], a[120:123], v[184:187], v[56:59]\n" \
  "v_exp_f32 v148, v148\n" \
  "v_mfma_f32_16x16x32_bf16 v[60:63], a[120:123], v[188:191], v[60:63]\n" \
  "v_exp_f32 v149, v149\n" \
  "v_mfma_f32_16x16x32_bf16 v[72:75], a[124:127], v[184:187], v[72:75]\n" \
  "v_exp_f32 v150, v150\n" \
  "v_mfma_f32_16x16x32_bf16 v[76:79], a[124:127], v[188:191], v[76:79]\n" \
  "v_exp_f32 v151, v151\n" \
  "v_mfma_f32_16x16x32_bf16 v[88:91], a[128:131], v[184:187], v[88:91]\n" \
  "v_exp_f32 v152, v152\n" \
  "v_mfma_f32_16x16x32_bf16 v[92:95], a[128:131], v[188:191], v[92:95]\n" \
  "v_exp_f32 v153, v153\n" \
  "v_mfma_f32_16x16x32_bf16 v[104:107], a[132:135], v[184:187], v[104:107]\n" \
  "v_exp_f32 v154, v154\n" \
  "v_mfma_f32_16x16x32_bf16 v[108:111], a[132:135], v[188:191], v[108:111]\n" \
  "v_exp_f32 v155, v155\n" \
  "v_mfma_f32_16x16x32_bf16 v[120:123], a[136:139], v[184:187], v[120:123]\n" \
  "v_exp_f32 v156, v156\n" \
  "v_mfma_f32_16x16x32_bf16 v[124:127], a[136:139], v[188:191], v[124:127]\n" \
  "v_exp_f32 v157, v157\n" \
  "v_mfma_f32_16x16x32_bf16 v[136:139], a[140:143], v[184:187], v[136:139]\n" \
  "v_exp_f32 v158, v158\n" \
  "v_mfma_f32_16x16x32_bf16 v[140:143], a[140:143], v[188:191], v[140:143]\n" \
  "v_exp_f32 v159, v159\n" \
  "v_mfma_f32_32x32x16_bf16 v[160:175], a[72:75], a[36:39], v[208:223]\n" \
  "ds_read_b64_tr_b16 a[108:109], v225 offset:0\n" \
  "ds_read_b64_tr_b16 a[110:111], v226 offset:0\n" \
  "ds_read_b64_tr_b16 a[112:113], v225 offset:1024\n" \
  "v_cvt_pk_bf16_f32 v176, v144, v145\n" \
  "v_cvt_pk_bf16_f32 v180, v152, v153\n" \
  "v_mfma_f32_32x32x16_bf16 v[160:175], a[76:79], a[40:43], v[160:175]\n" \
  "ds_read_b64_tr_b16 a[114:115], v226 offset:1024\n" \
  "ds_read_b64_tr_b16 a[116:117], v225 offset:2048\n" \
  "ds_read_b64_tr_b16 a[118:119], v226 offset:2048\n" \
  "v_cvt_pk_bf16_f32 v177, v146, v147\n" \
  "v_cvt_pk_bf16_f32 v181, v154, v155\n" \
  "v_mfma_f32_32x32x16_bf16 v[160:175], a[80:83], a[44:47], v[160:175]\n" \
  "buffer_load_dwordx4 a[184:187], v227, s[44:47], s80 offen offset:2048 sc1\n" \
  "ds_read_b64_tr_b16 a[120:121], v225 offset:3072\n" \
  "ds_read_b64_tr_b16 a[122:123], v226 offset:3072\n" \
  "v_cvt_pk_bf16_f32 v178, v148, v149\n" \
  "v_permlane16_swap_b32 v176, v180\n" \
  "v_mfma_f32_32x32x16_bf16 v[160:175], a[84:87], a[48:51], v[160:175]\n" \
  "ds_read_b64_tr_b16 a[124:125], v225 offset:4096\n" \
  "ds_read_b64_tr_b16 a[126:127], v226 offset:4096\n" \
  "ds_read_b64_tr_b16 a[128:129], v225 offset:5120\n" \
  "v_cvt_pk_bf16_f32 v182, v156, v157\n" \
  "v_permlane16_swap_b32 v177, v181\n" \
  "v_mfma_f32_32x32x16_bf16 v[160:175], a[88:91], a[52:55], v[160:175]\n" \
  "ds_read_b64_tr_b16 a[130:131], v226 offset:5120\n" \
  "ds_read_b64_tr_b16 a[132:133], v225 offset:6144\n" \
  "v_cvt_pk_bf16_f32 v179, v150, v151\n" \
  "v_cvt_pk_bf16_f32 v183, v158, v159\n" \
  "v_add_u32 v224, s40, v228\n" \
  "v_mfma_f32_32x32x16_bf16 v[160:175], a[92:95], a[56:59], v[160:175]\n" \
  "buffer_load_dwordx4 a[188:191], v227, s[44:47], s80 offen offset:3072 sc1\n" \
  "ds_read_b64_tr_b16 a[134:135], v226 offset:6144\n" \
  "ds_read_b64_tr_b16 a[136:137], v225 offset:7168\n" \
  "ds_read_b64_tr_b16 a[138:139], v226 offset:7168\n" \
  "v_permlane16_swap_b32 v178, v182\n" \
  "s_add_u32 s80, s80, 0x1000\n" \
  "v_mfma_f32_32x32x16_bf16 v[160:175], a[96:99], a[60:63], v[160:175]\n" \
  "ds_read_b64_tr_b16 a[140:141], v225 offset:8192\n" \
  "ds_read_b64_tr_b16 a[142:143], v226 offset:8192\n" \
  "v_permlane16_swap_b32 v179, v183\n" \
  "s_waitcnt vmcnt(12)\n" \
  "v_mfma_f32_32x32x16_bf16 v[160:175], a[100:103], a[64:67], v[160:175]\n" \
  "ds_read_b128 a[72:75], v224 offset:0\n" \
  "buffer_load_dwordx4 v231, s[36:39], s43 offen offset:3072 sc1 lds\n" \
  "s_add_u32 m0, m0, 0x1000\n" \
  "v_mfma_f32_32x32x16_bf16 v[160:175], a[104:107], a[68:71], v[160:175]\n" \
  "ds_read_b128 a[76:79], v224 offset:1024\n" \
  "buffer_load_dwordx4 v248, s[36:39], s43 offen offset:0 sc1 lds\n" \
  "s_add_u32 s43, s43, 9216\n" \
  "s_sub_u32 m0, m0, 45056\n" \
  "s_cselect_b32 m0, s55, m0\n" \
  "s_waitcnt lgkmcnt(2)\n" \
  "v_mfma_f32_16x16x32_bf16 v[0:3], a[108:111], v[176:179], v[0:3]\n" \
  "ds_read_b128 a[80:83], v224 offset:2048\n" \
  "v_mfma_f32_16x16x32_bf16 v[4:7], a[108:111], v[180:183], v[4:7]\n" \
  "ds_read_b128 a[84:87], v224 offset:3072\n" \
  "v_mfma_f32_16x16x32_bf16 v[16:19], a[112:115], v[176:179], v[16:19]\n" \
  "v_exp_f32 v160, v160\n" \
  "v_mfma_f32_16x16x32_bf16 v[20:23], a[112:115], v[180:183], v[20:23]\n" \
  "v_exp_f32 v161, v161\n" \
  "v_mfma_f32_16x16x32_bf16 v[32:35], a[116:119], v[176:179], v[32:35]\n" \
  "buffer_load_dwordx4 a[192:195], v227, s[44:47], s80 offen offset:0 sc1\n" \
  "v_exp_f32 v162, v162\n" \
  "v_mfma_f32_16x16x32_bf16 v[36:39], a[116:119], v[180:183], v[36:39]\n" \
  "v_exp_f32 v163, v163\n" \
  "v_mfma_f32_16x16x32_bf16 v[48:51], a[120:123], v[176:179], v[48:51]\n" \
  "v_exp_f32 v164, v164\n" \
  "v_mfma_f32_16x16x32_bf16 v[52:55], a[120:123], v[180:183], v[52:55]\n" \
  "v_exp_f32 v165, v165\n" \
  "v_mfma_f32_16x16x32_bf16 v[64:67], a[124:127], v[176:179], v[64:67]\n" \
  "v_exp_f32 v166, v166\n" \
  "v_mfma_f32_16x16x32_bf16 v[68:71], a[124:127], v[180:183], v[68:71]\n" \
  "v_exp_f32 v167, v167\n" \
  "v_mfma_f32_16x16x32_bf16 v[80:83], a[128:131], v[176:179], v[80:83]\n" \
  "v_exp_f32 v168, v168\n" \
  "v_mfma_f32_16x16x32_bf16 v[84:87], a[128:131], v[180:183], v[84:87]\n" \
  "v_exp_f32 v169, v169\n" \
  "v_mfma_f32_16x16x32_bf16 v[96:99], a[132:135], v[176:179], v[96:99]\n" \
  "buffer_load_dwordx4 a[196:199], v227, s[44:47], s80 offen offset:1024 sc1\n" \
  "v_exp_f32 v170, v170\n" \
  "v_mfma_f32_16x16x32_bf16 v[100:103], a[132:135], v[180:183], v[100:103]\n" \
  "v_exp_f32 v171, v171\n" \
  "v_mfma_f32_16x16x32_bf16 v[112:115], a[136:139], v[176:179], v[112:115]\n" \
  "v_exp_f32 v172, v172\n" \
  "v_mfma_f32_16x16x32_bf16 v[116:119], a[136:139], v[180:183], v[116:119]\n" \
  "v_exp_f32 v173, v173\n" \
  "v_mfma_f32_16x16x32_bf16 v[128:131], a[140:143], v[176:179], v[128:131]\n" \
  "v_exp_f32 v174, v174\n" \
  "v_mfma_f32_16x16x32_bf16 v[132:135], a[140:143], v[180:183], v[132:135]\n" \
  "v_exp_f32 v175, v175\n" \
  "s_waitcnt lgkmcnt(0)\n" \
  "v_mfma_f32_32x32x16_bf16 v[144:159], a[72:75], a[0:3], v[192:207]\n" \
  "ds_read_b128 a[88:91], v224 offset:4096\n" \
  "v_cvt_pk_bf16_f32 v184, v160, v161\n" \
  "v_cvt_pk_bf16_f32 v188, v168, v169\n" \
  "v_mfma_f32_32x32x16_bf16 v[144:159], a[76:79], a[4:7], v[144:159]\n" \
  "ds_read_b128 a[92:95], v224 offset:5120\n" \
  "buffer_load_dwordx4 v227, s[36:39], s43 offen offset:0 sc1 lds\n" \
  "v_cvt_pk_bf16_f32 v185, v162, v163\n" \
  "v_cvt_pk_bf16_f32 v189, v170, v171\n" \
  "v_mfma_f32_32x32x16_bf16 v[144:159], a[80:83], a[8:11], v[144:159]\n" \
  "buffer_load_dwordx4 a[200:203], v227, s[44:47], s80 offen offset:2048 sc1\n" \
  "ds_read_b128 a[96:99], v224 offset:6144\n" \
  "v_cvt_pk_bf16_f32 v186, v164, v165\n" \
  "v_permlane16_swap_b32 v184, v188\n" \
  "v_mfma_f32_32x32x16_bf16 v[144:159], a[84:87], a[12:15], v[144:159]\n" \
  "ds_read_b128 a[100:103], v224 offset:7168\n" \
  "buffer_load_dwordx4 v227, s[36:39], s43 offen offset:1024 sc1 lds\n" \
  "v_cvt_pk_bf16_f32 v190, v172, v173\n" \
  "v_permlane16_swap_b32 v185, v189\n" \
  "s_waitcnt lgkmcnt(2)\n" \
  "v_mfma_f32_32x32x16_bf16 v[144:159], a[88:91], a[16:19], v[144:159]\n" \
  "buffer_load_dwordx4 a[204:207], v227, s[44:47], s80 offen offset:3072 sc1\n" \
  "ds_read_b128 a[104:107], v224 offset:8192\n" \
  "v_cvt_pk_bf16_f32 v187, v166, v167\n" \
  "v_cvt_pk_bf16_f32 v191, v174, v175\n" \
  "s_add_u32 s80, s80, 0x1000\n" \
  "v_mfma_f32_32x32x16_bf16 v[144:159], a[92:95], a[20:23], v[144:159]\n" \
  "buffer_load_dwordx4 v227, s[36:39], s43 offen offset:2048 sc1 lds\n" \
  "v_permlane16_swap_b32 v186, v190\n" \
  "v_add_u32 v225, v224, v229\n" \
  "s_waitcnt lgkmcnt(1)\n" \
  "v_mfma_f32_32x32x16_bf16 v[144:159], a[96:99], a[24:27], v[144:159]\n" \
  "buffer_load_dwordx4 v227, s[36:39], s43 offen offset:3072 sc1 lds\n" \
  "v_permlane16_swap_b32 v187, v191\n" \
  "v_add_u32 v226, v224, v230\n" \
  "s_add_u32 m0, m0, 0x1000\n" \
  "v_mfma_f32_32x32x16_bf16 v[144:159], a[100:103], a[28:31], v[144:159]\n" \
  "buffer_load_dwordx4 v231, s[36:39], s43 offen offset:0 sc1 lds\n" \
  "s_sub_u32 s40, s40, 36864\n" \
  "s_cselect_b32 s40, 73728, s40\n" \
  "s_waitcnt lgkmcnt(0)\n" \
  "v_mfma_f32_32x32x16_bf16 v[144:159], a[104:107], a[32:35], v[144:159]\n" \
  "buffer_load_dwordx4 v231, s[36:39], s43 offen offset:1024 sc1 lds\n" \
  "v_mfma_f32_16x16x32_bf16 v[8:11], a[108:111], v[184:187], v[8:11]\n" \
  "buffer_load_dwordx4 v231, s[36:39], s43 offen offset:2048 sc1 lds\n" \
  "v_mfma_f32_16x16x32_bf16 v[12:15], a[108:111], v[188:191], v[12:15]\n" \
  "buffer_load_dwordx4 a[208:211], v227, s[44:47], s80 offen offset:0 sc1\n" \
  "v_mfma_f32_16x16x32_bf16 v[24:27], a[112:115], v[184:187], v[24:27]\n" \
  "v_exp_f32 v144, v144\n" \
  "v_mfma_f32_16x16x32_bf16 v[28:31], a[112:115], v[188:191], v[28:31]\n" \
  "v_exp_f32 v145, v145\n" \
  "v_mfma_f32_16x16x32_bf16 v[40:43], a[116:119], v[184:187], v[40:43]\n" \
  "v_exp_f32 v146, v146\n" \
  "v_mfma_f32_16x16x32_bf16 v[44:47], a[116:119], v[188:191], v[44:47]\n" \
  "v_exp_f32 v147, v147\n" \
  "v_mfma_f32_16x16x32_bf16 v[56:59], a[120:123], v[184:187], v[56:59]\n" \
  "v_exp_f32 v148, v148\n" \
  "v_mfma_f32_16x16x32_bf16 v[60:63], a[120:123], v[188:191], v[60:63]\n" \
  "v_exp_f32 v149, v149\n" \
  "v_mfma_f32_16x16x32_bf16 v[72:75], a[124:127], v[184:187], v[72:75]\n" \
  "v_exp_f32 v150, v150\n" \
  "v_mfma_f32_16x16x32_bf16 v[76:79], a[124:127], v[188:191], v[76:79]\n" \
  "v_exp_f32 v151, v151\n" \
  "v_mfma_f32_16x16x32_bf16 v[88:91], a[128:131], v[184:187], v[88:91]\n" \
  "v_exp_f32 v152, v152\n" \
  "v_mfma_f32_16x16x32_bf16 v[92:95], a[128:131], v[188:191], v[92:95]\n" \
  "v_exp_f32 v153, v153\n" \
  "v_mfma_f32_16x16x32_bf16 v[104:107], a[132:135], v[184:187], v[104:107]\n" \
  "v_exp_f32 v154, v154\n" \
  "v_mfma_f32_16x16x32_bf16 v[108:111], a[132:135], v[188:191], v[108:111]\n" \
  "v_exp_f32 v155, v155\n" \
  "v_mfma_f32_16x16x32_bf16 v[120:123], a[136:139], v[184:187], v[120:123]\n" \
  "v_exp_f32 v156, v156\n" \
  "v_mfma_f32_16x16x32_bf16 v[124:127], a[136:139], v[188:191], v[124:127]\n" \
  "v_exp_f32 v157, v157\n" \
  "v_mfma_f32_16x16x32_bf16 v[136:139], a[140:143], v[184:187], v[136:139]\n" \
  "v_exp_f32 v158, v158\n" \
  "v_mfma_f32_16x16x32_bf16 v[140:143], a[140:143], v[188:191], v[140:143]\n" \
  "v_exp_f32 v159, v159\n" \
  "v_mfma_f32_32x32x16_bf16 v[160:175], a[72:75], a[36:39], v[208:223]\n" \
  "ds_read_b64_tr_b16 a[108:109], v225 offset:0\n" \
  "ds_read_b64_tr_b16 a[110:111], v226 offset:0\n" \
  "ds_read_b64_tr_b16 a[112:113], v225 offset:1024\n" \
  "v_cvt_pk_bf16_f32 v176, v144, v145\n" \
  "v_cvt_pk_bf16_f32 v180, v152, v153\n" \
  "v_mfma_f32_32x32x16_bf16 v[160:175], a[76:79], a[40:43], v[160:175]\n" \
  "ds_read_b64_tr_b16 a[114:115], v226 offset:1024\n" \
  "ds_read_b64_tr_b16 a[116:117], v225 offset:2048\n" \
  "ds_read_b64_tr_b16 a[118:119], v226 offset:2048\n" \
  "v_cvt_pk_bf16_f32 v177, v146, v147\n" \
  "v_cvt_pk_bf16_f32 v181, v154, v155\n" \
  "v_mfma_f32_32x32x16_bf16 v[160:175], a[80:83], a[44:47], v[160:175]\n" \
  "buffer_load_dwordx4 a[212:215], v227, s[44:47], s80 offen offset:1024 sc1\n" \
  "ds_read_b64_tr_b16 a[120:121], v225 offset:3072\n" \
  "ds_read_b64_tr_b16 a[122:123], v226 offset:3072\n" \
  "v_cvt_pk_bf16_f32 v178, v148, v149\n" \
  "v_permlane16_swap_b32 v176, v180\n" \
  "v_mfma_f32_32x32x16_bf16 v[160:175], a[84:87], a[48:51], v[160:175]\n" \
  "ds_read_b64_tr_b16 a[124:125], v225 offset:4096\n" \
  "ds_read_b64_tr_b16 a[126:127], v226 offset:4096\n" \
  "ds_read_b64_tr_b16 a[128:129], v225 offset:5120\n" \
  "v_cvt_pk_bf16_f32 v182, v156, v157\n" \
  "v_permlane16_swap_b32 v177, v181\n" \
  "v_mfma_f32_32x32x16_bf16 v[160:175], a[88:91], a[52:55], v[160:175]\n" \
  "ds_read_b64_tr_b16 a[130:131], v226 offset:5120\n" \
  "ds_read_b64_tr_b16 a[132:133], v225 offset:6144\n" \
  "v_cvt_pk_bf16_f32 v179, v150, v151\n" \
  "v_cvt_pk_bf16_f32 v183, v158, v159\n" \
  "v_add_u32 v224, s40, v228\n" \
  "v_mfma_f32_32x32x16_bf16 v[160:175], a[92:95], a[56:59], v[160:175]\n" \
  "buffer_load_dwordx4 a[216:219], v227, s[44:47], s81 offen offset:0 sc1\n" \
  "ds_read_b64_tr_b16 a[134:135], v226 offset:6144\n" \
  "ds_read_b64_tr_b16 a[136:137], v225 offset:7168\n" \
  "ds_read_b64_tr_b16 a[138:139], v226 offset:7168\n" \
  "v_permlane16_swap_b32 v178, v182\n" \
  "v_mfma_f32_32x32x16_bf16 v[160:175], a[96:99], a[60:63], v[160:175]\n" \
  "ds_read_b64_tr_b16 a[140:141], v225 offset:8192\n" \
  "ds_read_b64_tr_b16 a[142:143], v226 offset:8192\n" \
  "v_permlane16_swap_b32 v179, v183\n" \
  "s_waitcnt vmcnt(12)\n" \
  "v_mfma_f32_32x32x16_bf16 v[160:175], a[100:103], a[64:67], v[160:175]\n" \
  "ds_read_b128 a[72:75], v224 offset:0\n" \
  "buffer_load_dwordx4 v231, s[36:39], s43 offen offset:3072 sc1 lds\n" \
  "s_add_u32 m0, m0, 0x1000\n" \
  "v_mfma_f32_32x32x16_bf16 v[160:175], a[104:107], a[68:71], v[160:175]\n" \
  "ds_read_b128 a[76:79], v224 offset:1024\n" \
  "buffer_load_dwordx4 v248, s[36:39], s43 offen offset:0 sc1 lds\n" \
  "s_add_u32 s43, s43, 9216\n" \
  "s_sub_u32 m0, m0, 45056\n" \
  "s_cselect_b32 m0, s55, m0\n" \
  "s_waitcnt lgkmcnt(2)\n" \
  "v_mfma_f32_16x16x32_bf16 v[0:3], a[108:111], v[176:179], v[0:3]\n" \
  "ds_read_b128 a[80:83], v224 offset:2048\n" \
  "v_mfma_f32_16x16x32_bf16 v[4:7], a[108:111], v[180:183], v[4:7]\n" \
  "ds_read_b128 a[84:87], v224 offset:3072\n" \
  "v_mfma_f32_16x16x32_bf16 v[16:19], a[112:115], v[176:179], v[16:19]\n" \
  "v_exp_f32 v160, v160\n" \
  "v_mfma_f32_16x16x32_bf16 v[20:23], a[112:115], v[180:183], v[20:23]\n" \
  "v_exp_f32 v161, v161\n" \
  "v_mfma_f32_16x16x32_bf16 v[32:35], a[116:119], v[176:179], v[32:35]\n" \
  "buffer_load_dwordx4 a[220:223], v227, s[44:47], s81 offen offset:1024 sc1\n" \
  "v_exp_f32 v162, v162\n" \
  "v_mfma_f32_16x16x32_bf16 v[36:39], a[116:119], v[180:183], v[36:39]\n" \
  "v_exp_f32 v163, v163\n" \
  "v_mfma_f32_16x16x32_bf16 v[48:51], a[120:123], v[176:179], v[48:51]\n" \
  "v_exp_f32 v164, v164\n" \
  "v_mfma_f32_16x16x32_bf16 v[52:55], a[120:123], v[180:183], v[52:55]\n" \
  "v_exp_f32 v165, v165\n" \
  "v_mfma_f32_16x16x32_bf16 v[64:67], a[124:127], v[176:179], v[64:67]\n" \
  "v_exp_f32 v166, v166\n" \
  "v_mfma_f32_16x16x32_bf16 v[68:71], a[124:127], v[180:183], v[68:71]\n" \
  "v_exp_f32 v167, v167\n" \
  "v_mfma_f32_16x16x32_bf16 v[80:83], a[128:131], v[176:179], v[80:83]\n" \
  "v_exp_f32 v168, v168\n" \
  "v_mfma_f32_16x16x32_bf16 v[84:87], a[128:131], v[180:183], v[84:87]\n" \
  "v_exp_f32 v169, v169\n" \
  "v_mfma_f32_16x16x32_bf16 v[96:99], a[132:135], v[176:179], v[96:99]\n" \
  "buffer_load_dwordx4 a[224:227], v227, s[44:47], s81 offen offset:2048 sc1\n" \
  "v_exp_f32 v170, v170\n" \
  "v_mfma_f32_16x16x32_bf16 v[100:103], a[132:135], v[180:183], v[100:103]\n" \
  "v_exp_f32 v171, v171\n" \
  "v_mfma_f32_16x16x32_bf16 v[112:115], a[136:139], v[176:179], v[112:115]\n" \
  "v_exp_f32 v172, v172\n" \
  "v_mfma_f32_16x16x32_bf16 v[116:119], a[136:139], v[180:183], v[116:119]\n" \
  "v_exp_f32 v173, v173\n" \
  "v_mfma_f32_16x16x32_bf16 v[128:131], a[140:143], v[176:179], v[128:131]\n" \
  "v_exp_f32 v174, v174\n" \
  "v_mfma_f32_16x16x32_bf16 v[132:135], a[140:143], v[180:183], v[132:135]\n" \
  "v_exp_f32 v175, v175\n" \
  "s_waitcnt lgkmcnt(0)\n" \
  "v_mfma_f32_32x32x16_bf16 v[144:159], a[72:75], a[0:3], v[192:207]\n" \
  "ds_read_b128 a[88:91], v224 offset:4096\n" \
  "v_cvt_pk_bf16_f32 v184, v160, v161\n" \
  "v_cvt_pk_bf16_f32 v188, v168, v169\n" \
  "v_mfma_f32_32x32x16_bf16 v[144:159], a[76:79], a[4:7], v[144:159]\n" \
  "ds_read_b128 a[92:95], v224 offset:5120\n" \
  "buffer_load_dwordx4 v227, s[36:39], s43 offen offset:0 sc1 lds\n" \
  "v_cvt_pk_bf16_f32 v185, v162, v163\n" \
  "v_cvt_pk_bf16_f32 v189, v170, v171\n" \
  "v_mfma_f32_32x32x16_bf16 v[144:159], a[80:83], a[8:11], v[144:159]\n" \
  "buffer_load_dwordx4 a[228:231], v227, s[44:47], s81 offen offset:3072 sc1\n" \
  "ds_read_b128 a[96:99], v224 offset:6144\n" \
  "v_cvt_pk_bf16_f32 v186, v164, v165\n" \
  "v_permlane16_swap_b32 v184, v188\n" \
  "s_add_u32 s81, s81, 0x1000\n" \
  "v_mfma_f32_32x32x16_bf16 v[144:159], a[84:87], a[12:15], v[144:159]\n" \
  "ds_read_b128 a[100:103], v224 offset:7168\n" \
  "buffer_load_dwordx4 v227, s[36:39], s43 offen offset:1024 sc1 lds\n" \
  "v_cvt_pk_bf16_f32 v190, v172, v173\n" \
  "v_permlane16_swap_b32 v185, v189\n" \
  "s_waitcnt lgkmcnt(2)\n" \
  "v_mfma_f32_32x32x16_bf16 v[144:159], a[88:91], a[16:19], v[144:159]\n" \
  "buffer_load_dwordx4 a[232:235], v227, s[44:47], s81 offen offset:0 sc1\n" \
  "ds_read_b128 a[104:107], v224 offset:8192\n" \
  "v_cvt_pk_bf16_f32 v187, v166, v167\n" \
  "v_cvt_pk_bf16_f32 v191, v174, v175\n" \
  "v_mfma_f32_32x32x16_bf16 v[144:159], a[92:95], a[20:23], v[144:159]\n" \
  "buffer_load_dwordx4 v227, s[36:39], s43 offen offset:2048 sc1 lds\n" \
  "v_permlane16_swap_b32 v186, v190\n" \
  "v_add_u32 v225, v224, v229\n" \
  "s_waitcnt lgkmcnt(1)\n" \
  "v_mfma_f32_32x32x16_bf16 v[144:159], a[96:99], a[24:27], v[144:159]\n" \
  "buffer_load_dwordx4 v227, s[36:39], s43 offen offset:3072 sc1 lds\n" \
  "v_permlane16_swap_b32 v187, v191\n" \
  "v_add_u32 v226, v224, v230\n" \
  "s_add_u32 m0, m0, 0x1000\n" \
  "v_mfma_f32_32x32x16_bf16 v[144:159], a[100:103], a[28:31], v[144:159]\n" \
  "buffer_load_dwordx4 v231, s[36:39], s43 offen offset:0 sc1 lds\n" \
  "s_sub_u32 s40, s40, 36864\n" \
  "s_cselect_b32 s40, 73728, s40\n" \
  "s_waitcnt lgkmcnt(0)\n" \
  "v_mfma_f32_32x32x16_bf16 v[144:159], a[104:107], a[32:35], v[144:159]\n" \
  "buffer_load_dwordx4 v231, s[36:39], s43 offen offset:1024 sc1 lds\n" \
  "v_mfma_f32_16x16x32_bf16 v[8:11], a[108:111], v[184:187], v[8:11]\n" \
  "buffer_load_dwordx4 v231, s[36:39], s43 offen offset:2048 sc1 lds\n" \
  "v_mfma_f32_16x16x32_bf16 v[12:15], a[108:111], v[188:191], v[12:15]\n" \
  "buffer_load_dwordx4 a[236:239], v227, s[44:47], s81 offen offset:1024 sc1\n" \
  "v_mfma_f32_16x16x32_bf16 v[24:27], a[112:115], v[184:187], v[24:27]\n" \
  "v_exp_f32 v144, v144\n" \
  "v_mfma_f32_16x16x32_bf16 v[28:31], a[112:115], v[188:191], v[28:31]\n" \
  "v_exp_f32 v145, v145\n" \
  "v_mfma_f32_16x16x32_bf16 v[40:43], a[116:119], v[184:187], v[40:43]\n" \
  "v_exp_f32 v146, v146\n" \
  "v_mfma_f32_16x16x32_bf16 v[44:47], a[116:119], v[188:191], v[44:47]\n" \
  "v_exp_f32 v147, v147\n" \
  "v_mfma_f32_16x16x32_bf16 v[56:59], a[120:123], v[184:187], v[56:59]\n" \
  "v_exp_f32 v148, v148\n" \
  "v_mfma_f32_16x16x32_bf16 v[60:63], a[120:123], v[188:191], v[60:63]\n" \
  "v_exp_f32 v149, v149\n" \
  "v_mfma_f32_16x16x32_bf16 v[72:75], a[124:127], v[184:187], v[72:75]\n" \
  "v_exp_f32 v150, v150\n" \
  "v_mfma_f32_16x16x32_bf16 v[76:79], a[124:127], v[188:191], v[76:79]\n" \
  "v_exp_f32 v151, v151\n" \
  "v_mfma_f32_16x16x32_bf16 v[88:91], a[128:131], v[184:187], v[88:91]\n" \
  "v_exp_f32 v152, v152\n" \
  "v_mfma_f32_16x16x32_bf16 v[92:95], a[128:131], v[188:191], v[92:95]\n" \
  "v_exp_f32 v153, v153\n" \
  "v_mfma_f32_16x16x32_bf16 v[104:107], a[132:135], v[184:187], v[104:107]\n" \
  "v_exp_f32 v154, v154\n" \
  "v_mfma_f32_16x16x32_bf16 v[108:111], a[132:135], v[188:191], v[108:111]\n" \
  "v_exp_f32 v155, v155\n" \
  "v_mfma_f32_16x16x32_bf16 v[120:123], a[136:139], v[184:187], v[120:123]\n" \
  "v_exp_f32 v156, v156\n" \
  "v_mfma_f32_16x16x32_bf16 v[124:127], a[136:139], v[188:191], v[124:127]\n" \
  "v_exp_f32 v157, v157\n" \
  "v_mfma_f32_16x16x32_bf16 v[136:139], a[140:143], v[184:187], v[136:139]\n" \
  "v_exp_f32 v158, v158\n" \
  "v_mfma_f32_16x16x32_bf16 v[140:143], a[140:143], v[188:191], v[140:143]\n" \
  "v_exp_f32 v159, v159\n" \
  "v_mfma_f32_32x32x16_bf16 v[160:175], a[72:75], a[36:39], v[208:223]\n" \
  "ds_read_b64_tr_b16 a[108:109], v225 offset:0\n" \
  "ds_read_b64_tr_b16 a[110:111], v226 offset:0\n" \
  "ds_read_b64_tr_b16 a[112:113], v225 offset:1024\n" \
  "v_cvt_pk_bf16_f32 v176, v144, v145\n" \
  "v_cvt_pk_bf16_f32 v180, v152, v153\n" \
  "v_mfma_f32_32x32x16_bf16 v[160:175], a[76:79], a[40:43], v[160:175]\n" \
  "ds_read_b64_tr_b16 a[114:115], v226 offset:1024\n" \
  "ds_read_b64_tr_b16 a[116:117], v225 offset:2048\n" \
  "ds_read_b64_tr_b16 a[118:119], v226 offset:2048\n" \
  "v_cvt_pk_bf16_f32 v177, v146, v147\n" \
  "v_cvt_pk_bf16_f32 v181, v154, v155\n" \
  "v_mfma_f32_32x32x16_bf16 v[160:175], a[80:83], a[44:47], v[160:175]\n" \
  "buffer_load_dwordx4 a[240:243], v227, s[44:47], s81 offen offset:2048 sc1\n" \
  "ds_read_b64_tr_b16 a[120:121], v225 offset:3072\n" \
  "ds_read_b64_tr_b16 a[122:123], v226 offset:3072\n" \
  "v_cvt_pk_bf16_f32 v178, v148, v149\n" \
  "v_permlane16_swap_b32 v176, v180\n" \
  "v_mfma_f32_32x32x16_bf16 v[160:175], a[84:87], a[48:51], v[160:175]\n" \
  "ds_read_b64_tr_b16 a[124:125], v225 offset:4096\n" \
  "ds_read_b64_tr_b16 a[126:127], v226 offset:4096\n" \
  "ds_read_b64_tr_b16 a[128:129], v225 offset:5120\n" \
  "v_cvt_pk_bf16_f32 v182, v156, v157\n" \
  "v_permlane16_swap_b32 v177, v181\n" \
  "v_mfma_f32_32x32x16_bf16 v[160:175], a[88:91], a[52:55], v[160:175]\n" \
  "ds_read_b64_tr_b16 a[130:131], v226 offset:5120\n" \
  "ds_read_b64_tr_b16 a[132:133], v225 offset:6144\n" \
  "v_cvt_pk_bf16_f32 v179, v150, v151\n" \
  "v_cvt_pk_bf16_f32 v183, v158, v159\n" \
  "v_add_u32 v224, s40, v228\n" \
  "v_mfma_f32_32x32x16_bf16 v[160:175], a[92:95], a[56:59], v[160:175]\n" \
  "buffer_load_dwordx4 a[244:247], v227, s[44:47], s81 offen offset:3072 sc1\n" \
  "ds_read_b64_tr_b16 a[134:135], v226 offset:6144\n" \
  "ds_read_b64_tr_b16 a[136:137], v225 offset:7168\n" \
  "ds_read_b64_tr_b16 a[138:139], v226 offset:7168\n" \
  "v_permlane16_swap_b32 v178, v182\n" \
  "s_add_u32 s81, s81, 0x1000\n" \
  "v_mfma_f32_32x32x16_bf16 v[160:175], a[96:99], a[60:63], v[160:175]\n" \
  "ds_read_b64_tr_b16 a[140:141], v225 offset:8192\n" \
  "ds_read_b64_tr_b16 a[142:143], v226 offset:8192\n" \
  "v_permlane16_swap_b32 v179, v183\n" \
  "s_waitcnt vmcnt(12)\n" \
  "v_mfma_f32_32x32x16_bf16 v[160:175], a[100:103], a[64:67], v[160:175]\n" \
  "ds_read_b128 a[72:75], v224 offset:0\n" \
  "buffer_load_dwordx4 v231, s[36:39], s43 offen offset:3072 sc1 lds\n" \
  "s_add_u32 m0, m0, 0x1000\n" \
  "v_mfma_f32_32x32x16_bf16 v[160:175], a[104:107], a[68:71], v[160:175]\n" \
  "ds_read_b128 a[76:79], v224 offset:1024\n" \
  "buffer_load_dwordx4 v248, s[36:39], s43 offen offset:0 sc1 lds\n" \
  "s_add_u32 s43, s43, 9216\n" \
  "s_sub_u32 m0, m0, 45056\n" \
  "s_cselect_b32 m0, s55, m0\n" \
  "s_waitcnt lgkmcnt(2)\n" \
  "v_mfma_f32_16x16x32_bf16 v[0:3], a[108:111], v[176:179], v[0:3]\n" \
  "ds_read_b128 a[80:83], v224 offset:2048\n" \
  "v_mfma_f32_16x16x32_bf16 v[4:7], a[108:111], v[180:183], v[4:7]\n" \
  "ds_read_b128 a[84:87], v224 offset:3072\n" \
  "v_mfma_f32_16x16x32_bf16 v[16:19], a[112:115], v[176:179], v[16:19]\n" \
  "v_exp_f32 v160, v160\n" \
  "v_mfma_f32_16x16x32_bf16 v[20:23], a[112:115], v[180:183], v[20:23]\n" \
  "v_exp_f32 v161, v161\n" \
  "v_mfma_f32_16x16x32_bf16 v[32:35], a[116:119], v[176:179], v[32:35]\n" \
  "buffer_load_dwordx4 a[248:251], v227, s[44:47], s81 offen offset:0 sc1\n" \
  "v_exp_f32 v162, v162\n" \
  "v_mfma_f32_16x16x32_bf16 v[36:39], a[116:119], v[180:183], v[36:39]\n" \
  "v_exp_f32 v163, v163\n" \
  "v_mfma_f32_16x16x32_bf16 v[48:51], a[120:123], v[176:179], v[48:51]\n" \
  "v_exp_f32 v164, v164\n" \
  "v_mfma_f32_16x16x32_bf16 v[52:55], a[120:123], v[180:183], v[52:55]\n" \
  "v_exp_f32 v165, v165\n" \
  "v_mfma_f32_16x16x32_bf16 v[64:67], a[124:127], v[176:179], v[64:67]\n" \
  "v_exp_f32 v166, v166\n" \
  "v_mfma_f32_16x16x32_bf16 v[68:71], a[124:127], v[180:183], v[68:71]\n" \
  "v_exp_f32 v167, v167\n" \
  "v_mfma_f32_16x16x32_bf16 v[80:83], a[128:131], v[176:179], v[80:83]\n" \
  "v_exp_f32 v168, v168\n" \
  "v_mfma_f32_16x16x32_bf16 v[84:87], a[128:131], v[180:183], v[84:87]\n" \
  "v_exp_f32 v169, v169\n" \
  "v_mfma_f32_16x16x32_bf16 v[96:99], a[132:135], v[176:179], v[96:99]\n" \
  "v_exp_f32 v170, v170\n" \
  "v_mfma_f32_16x16x32_bf16 v[100:103], a[132:135], v[180:183], v[100:103]\n" \
  "v_exp_f32 v171, v171\n" \
  "v_mfma_f32_16x16x32_bf16 v[112:115], a[136:139], v[176:179], v[112:115]\n" \
  "v_exp_f32 v172, v172\n" \
  "v_mfma_f32_16x16x32_bf16 v[116:119], a[136:139], v[180:183], v[116:119]\n" \
  "v_exp_f32 v173, v173\n" \
  "v_mfma_f32_16x16x32_bf16 v[128:131], a[140:143], v[176:179], v[128:131]\n" \
  "v_exp_f32 v174, v174\n" \
  "v_mfma_f32_16x16x32_bf16 v[132:135], a[140:143], v[180:183], v[132:135]\n" \
  "v_exp_f32 v175, v175\n" \
  "s_waitcnt lgkmcnt(0)\n" \
  "v_mfma_f32_32x32x16_bf16 v[144:159], a[72:75], a[0:3], v[192:207]\n" \
  "ds_read_b128 a[88:91], v224 offset:4096\n" \
  "v_cvt_pk_bf16_f32 v184, v160, v161\n" \
  "v_cvt_pk_bf16_f32 v188, v168, v169\n" \
  "s_sub_u32 s50, %[t1], %[t0]\n" \
  "s_sub_u32 s50, s50, 7\n" \
  ".p2align 6\n" \
  "LOOP%=:\n" \
  "v_mfma_f32_32x32x16_bf16 v[144:159], a[76:79], a[4:7], v[144:159]\n" \
  "ds_read_b128 a[92:95], v224 offset:5120\n" \
  "buffer_load_dwordx4 v227, s[36:39], s43 offen offset:0 sc1 lds\n" \
  "v_cvt_pk_bf16_f32 v185, v162, v163\n" \
  "v_cvt_pk_bf16_f32 v189, v170, v171\n" \
  "v_mfma_f32_32x32x16_bf16 v[144:159], a[80:83], a[8:11], v[144:159]\n" \
  "ds_read_b128 a[96:99], v224 offset:6144\n" \
  "v_cvt_pk_bf16_f32 v186, v164, v165\n" \
  "v_permlane16_swap_b32 v184, v188\n" \
  "v_mfma_f32_32x32x16_bf16 v[144:159], a[84:87], a[12:15], v[144:159]\n" \
  "ds_read_b128 a[100:103], v224 offset:7168\n" \
  "buffer_load_dwordx4 v227, s[36:39], s43 offen offset:1024 sc1 lds\n" \
  "v_cvt_pk_bf16_f32 v190, v172, v173\n" \
  "v_permlane16_swap_b32 v185, v189\n" \
  "ATA%=:\n" \
  "s_waitcnt lgkmcnt(2)\n" \
  "v_mfma_f32_32x32x16_bf16 v[144:159], a[88:91], a[16:19], v[144:159]\n" \
  "ds_read_b128 a[104:107], v224 offset:8192\n" \
  "v_cvt_pk_bf16_f32 v187, v166, v167\n" \
  "v_cvt_pk_bf16_f32 v191, v174, v175\n" \
  "v_mfma_f32_32x32x16_bf16 v[144:159], a[92:95], a[20:23], v[144:159]\n" \
  "buffer_load_dwordx4 v227, s[36:39], s43 offen offset:2048 sc1 lds\n" \
  "v_permlane16_swap_b32 v186, v190\n" \
  "v_add_u32 v225, v224, v229\n" \
  "s_waitcnt lgkmcnt(1)\n" \
  "v_mfma_f32_32x32x16_bf16 v[144:159], a[96:99], a[24:27], v[144:159]\n" \
  "buffer_load_dwordx4 v227, s[36:39], s43 offen offset:3072 sc1 lds\n" \
  "v_permlane16_swap_b32 v187, v191\n" \
  "v_add_u32 v226, v224, v230\n" \
  "s_add_u32 m0, m0, 0x1000\n" \
  "v_mfma_f32_32x32x16_bf16 v[144:159], a[100:103], a[28:31], v[144:159]\n" \
  "buffer_load_dwordx4 v231, s[36:39], s43 offen offset:0 sc1 lds\n" \
  "s_sub_u32 s40, s40, 36864\n" \
  "s_cselect_b32 s40, 73728, s40\n" \
  "s_waitcnt lgkmcnt(0)\n" \
  "v_mfma_f32_32x32x16_bf16 v[144:159], a[104:107], a[32:35], v[144:159]\n" \
  "buffer_load_dwordx4 v231, s[36:39], s43 offen offset:1024 sc1 lds\n" \
  "v_mfma_f32_16x16x32_bf16 v[8:11], a[108:111], v[184:187], v[8:11]\n" \
  "buffer_load_dwordx4 v231, s[36:39], s43 offen offset:2048 sc1 lds\n" \
  "v_mfma_f32_16x16x32_bf16 v[12:15], a[108:111], v[188:191], v[12:15]\n" \
  "v_mfma_f32_16x16x32_bf16 v[24:27], a[112:115], v[184:187], v[24:27]\n" \
  "v_exp_f32 v144, v144\n" \
  "v_mfma_f32_16x16x32_bf16 v[28:31], a[112:115], v[188:191], v[28:31]\n" \
  "v_exp_f32 v145, v145\n" \
  "v_mfma_f32_16x16x32_bf16 v[40:43], a[116:119], v[184:187], v[40:43]\n" \
  "v_exp_f32 v146, v146\n" \
  "v_mfma_f32_16x16x32_bf16 v[44:47], a[116:119], v[188:191], v[44:47]\n" \
  "v_exp_f32 v147, v147\n" \
  "v_mfma_f32_16x16x32_bf16 v[56:59], a[120:123], v[184:187], v[56:59]\n" \
  "v_exp_f32 v148, v148\n" \
  "v_mfma_f32_16x16x32_bf16 v[60:63], a[120:123], v[188:191], v[60:63]\n" \
  "v_exp_f32 v149, v149\n" \
  "v_mfma_f32_16x16x32_bf16 v[72:75], a[124:127], v[184:187], v[72:75]\n" \
  "v_exp_f32 v150, v150\n" \
  "v_mfma_f32_16x16x32_bf16 v[76:79], a[124:127], v[188:191], v[76:79]\n" \
  "v_exp_f32 v151, v151\n" \
  "v_mfma_f32_16x16x32_bf16 v[88:91], a[128:131], v[184:187], v[88:91]\n" \
  "v_exp_f32 v152, v152\n" \
  "v_mfma_f32_16x16x32_bf16 v[92:95], a[128:131], v[188:191], v[92:95]\n" \
  "v_exp_f32 v153, v153\n" \
  "v_mfma_f32_16x16x32_bf16 v[104:107], a[132:135], v[184:187], v[104:107]\n" \
  "v_exp_f32 v154, v154\n" \
  "v_mfma_f32_16x16x32_bf16 v[108:111], a[132:135], v[188:191], v[108:111]\n" \
  "v_exp_f32 v155, v155\n" \
  "v_mfma_f32_16x16x32_bf16 v[120:123], a[136:139], v[184:187], v[120:123]\n" \
  "v_exp_f32 v156, v156\n" \
  "v_mfma_f32_16x16x32_bf16 v[124:127], a[136:139], v[188:191], v[124:127]\n" \
  "v_exp_f32 v157, v157\n" \
  "v_mfma_f32_16x16x32_bf16 v[136:139], a[140:143], v[184:187], v[136:139]\n" \
  "v_exp_f32 v158, v158\n" \
  "v_mfma_f32_16x16x32_bf16 v[140:143], a[140:143], v[188:191], v[140:143]\n" \
  "v_exp_f32 v159, v159\n" \
  "NOB%=:\n" \
  "v_mfma_f32_32x32x16_bf16 v[160:175], a[72:75], a[36:39], v[208:223]\n" \
  "ds_read_b64_tr_b16 a[108:109], v225 offset:0\n" \
  "ds_read_b64_tr_b16 a[110:111], v226 offset:0\n" \
  "ds_read_b64_tr_b16 a[112:113], v225 offset:1024\n" \
  "v_cvt_pk_bf16_f32 v176, v144, v145\n" \
  "v_cvt_pk_bf16_f32 v180, v152, v153\n" \
  "v_mfma_f32_32x32x16_bf16 v[160:175], a[76:79], a[40:43], v[160:175]\n" \
  "ds_read_b64_tr_b16 a[114:115], v226 offset:1024\n" \
  "ds_read_b64_tr_b16 a[116:117], v225 offset:2048\n" \
  "ds_read_b64_tr_b16 a[118:119], v226 offset:2048\n" \
  "v_cvt_pk_bf16_f32 v177, v146, v147\n" \
  "v_cvt_pk_bf16_f32 v181, v154, v155\n" \
  "v_mfma_f32_32x32x16_bf16 v[160:175], a[80:83], a[44:47], v[160:175]\n" \
  "ds_read_b64_tr_b16 a[120:121], v225 offset:3072\n" \
  "ds_read_b64_tr_b16 a[122:123], v226 offset:3072\n" \
  "v_cvt_pk_bf16_f32 v178, v148, v149\n" \
  "v_permlane16_swap_b32 v176, v180\n" \
  "v_mfma_f32_32x32x16_bf16 v[160:175], a[84:87], a[48:51], v[160:175]\n" \
  "ds_read_b64_tr_b16 a[124:125], v225 offset:4096\n" \
  "ds_read_b64_tr_b16 a[126:127], v226 offset:4096\n" \
  "ds_read_b64_tr_b16 a[128:129], v225 offset:5120\n" \
  "v_cvt_pk_bf16_f32 v182, v156, v157\n" \
  "v_permlane16_swap_b32 v177, v181\n" \
  "v_mfma_f32_32x32x16_bf16 v[160:175], a[88:91], a[52:55], v[160:175]\n" \
  "ds_read_b64_tr_b16 a[130:131], v226 offset:5120\n" \
  "ds_read_b64_tr_b16 a[132:133], v225 offset:6144\n" \
  "v_cvt_pk_bf16_f32 v179, v150, v151\n" \
  "v_cvt_pk_bf16_f32 v183, v158, v159\n" \
  "v_add_u32 v224, s40, v228\n" \
  "v_mfma_f32_32x32x16_bf16 v[160:175], a[92:95], a[56:59], v[160:175]\n" \
  "ds_read_b64_tr_b16 a[134:135], v226 offset:6144\n" \
  "ds_read_b64_tr_b16 a[136:137], v225 offset:7168\n" \
  "ds_read_b64_tr_b16 a[138:139], v226 offset:7168\n" \
  "v_permlane16_swap_b32 v178, v182\n" \
  "v_mfma_f32_32x32x16_bf16 v[160:175], a[96:99], a[60:63], v[160:175]\n" \
  "ds_read_b64_tr_b16 a[140:141], v225 offset:8192\n" \
  "ds_read_b64_tr_b16 a[142:143], v226 offset:8192\n" \
  "v_permlane16_swap_b32 v179, v183\n" \
  "s_waitcnt vmcnt(7)\n" \
  "v_mfma_f32_32x32x16_bf16 v[160:175], a[100:103], a[64:67], v[160:175]\n" \
  "ds_read_b128 a[72:75], v224 offset:0\n" \
  "buffer_load_dwordx4 v231, s[36:39], s43 offen offset:3072 sc1 lds\n" \
  "s_add_u32 m0, m0, 0x1000\n" \
  "v_mfma_f32_32x32x16_bf16 v[160:175], a[104:107], a[68:71], v[160:175]\n" \
  "ds_read_b128 a[76:79], v224 offset:1024\n" \
  "buffer_load_dwordx4 v248, s[36:39], s43 offen offset:0 sc1 lds\n" \
  "s_add_u32 s43, s43, 9216\n" \
  "s_sub_u32 m0, m0, 45056\n" \
  "s_cselect_b32 m0, s55, m0\n" \
  "ATD%=:\n" \
  "s_waitcnt lgkmcnt(2)\n" \
  "v_mfma_f32_16x16x32_bf16 v[0:3], a[108:111], v[176:179], v[0:3]\n" \
  "ds_read_b128 a[80:83], v224 offset:2048\n" \
  "v_mfma_f32_16x16x32_bf16 v[4:7], a[108:111], v[180:183], v[4:7]\n" \
  "ds_read_b128 a[84:87], v224 offset:3072\n" \
  "v_mfma_f32_16x16x32_bf16 v[16:19], a[112:115], v[176:179], v[16:19]\n" \
  "v_exp_f32 v160, v160\n" \
  "v_mfma_f32_16x16x32_bf16 v[20:23], a[112:115], v[180:183], v[20:23]\n" \
  "v_exp_f32 v161, v161\n" \
  "v_mfma_f32_16x16x32_bf16 v[32:35], a[116:119], v[176:179], v[32:35]\n" \
  "v_exp_f32 v162, v162\n" \
  "v_mfma_f32_16x16x32_bf16 v[36:39], a[116:119], v[180:183], v[36:39]\n" \
  "v_exp_f32 v163, v163\n" \
  "v_mfma_f32_16x16x32_bf16 v[48:51], a[120:123], v[176:179], v[48:51]\n" \
  "v_exp_f32 v164, v164\n" \
  "v_mfma_f32_16x16x32_bf16 v[52:55], a[120:123], v[180:183], v[52:55]\n" \
  "v_exp_f32 v165, v165\n" \
  "v_mfma_f32_16x16x32_bf16 v[64:67], a[124:127], v[176:179], v[64:67]\n" \
  "v_exp_f32 v166, v166\n" \
  "v_mfma_f32_16x16x32_bf16 v[68:71], a[124:127], v[180:183], v[68:71]\n" \
  "v_exp_f32 v167, v167\n" \
  "v_mfma_f32_16x16x32_bf16 v[80:83], a[128:131], v[176:179], v[80:83]\n" \
  "v_exp_f32 v168, v168\n" \
  "v_mfma_f32_16x16x32_bf16 v[84:87], a[128:131], v[180:183], v[84:87]\n" \
  "v_exp_f32 v169, v169\n" \
  "v_mfma_f32_16x16x32_bf16 v[96:99], a[132:135], v[176:179], v[96:99]\n" \
  "v_exp_f32 v170, v170\n" \
  "v_mfma_f32_16x16x32_bf16 v[100:103], a[132:135], v[180:183], v[100:103]\n" \
  "v_exp_f32 v171, v171\n" \
  "v_mfma_f32_16x16x32_bf16 v[112:115], a[136:139], v[176:179], v[112:115]\n" \
  "v_exp_f32 v172, v172\n" \
  "v_mfma_f32_16x16x32_bf16 v[116:119], a[136:139], v[180:183], v[116:119]\n" \
  "v_exp_f32 v173, v173\n" \
  "v_mfma_f32_16x16x32_bf16 v[128:131], a[140:143], v[176:179], v[128:131]\n" \
  "v_exp_f32 v174, v174\n" \
  "v_mfma_f32_16x16x32_bf16 v[132:135], a[140:143], v[180:183], v[132:135]\n" \
  "v_exp_f32 v175, v175\n" \
  "s_waitcnt lgkmcnt(0)\n" \
  "v_mfma_f32_32x32x16_bf16 v[144:159], a[72:75], a[0:3], v[192:207]\n" \
  "ds_read_b128 a[88:91], v224 offset:4096\n" \
  "v_cvt_pk_bf16_f32 v184, v160, v161\n" \
  "v_cvt_pk_bf16_f32 v188, v168, v169\n" \
  "s_sub_u32 s50, s50, 1\n" \
  "s_cbranch_scc0 LOOP%=\n" \
  "s_cmp_lg_u32 s52, 0\n" \
  "s_cbranch_scc1 EXIT%=\n" \
  "buffer_load_dwordx2 v[232:233], v235, s[44:47], 0 offen sc1\n" \
  "s_mov_b32 s52, 1\n" \
  "s_mov_b32 s50, 0\n" \
  "s_branch LOOP%=\n" \
  "TP%=:\n" \
  "v_cndmask_b32 v170, 0, v164, s[68:69]\n" \
  "v_pk_add_f32 v[172:173], v[0:1], v[164:165] op_sel_hi:[1,0] neg_lo:[0,1] neg_hi:[0,1]\n" \
  "v_pk_mul_f32 v[44:45], v[172:173], v[172:173]\n" \
  "v_pk_add_f32 v[172:173], v[2:3], v[164:165] op_sel_hi:[1,0] neg_lo:[0,1] neg_hi:[0,1]\n" \
  "v_pk_fma_f32 v[44:45], v[172:173], v[172:173], v[44:45]\n" \
  "v_pk_add_f32 v[172:173], v[4:5], v[164:165] op_sel_hi:[1,0] neg_lo:[0,1] neg_hi:[0,1]\n" \
  "v_pk_fma_f32 v[44:45], v[172:173], v[172:173], v[44:45]\n" \
  "v_pk_add_f32 v[172:173], v[6:7], v[164:165] op_sel_hi:[1,0] neg_lo:[0,1] neg_hi:[0,1]\n" \
  "v_pk_fma_f32 v[44:45], v[172:173], v[172:173], v[44:45]\n" \
  "v_pk_add_f32 v[172:173], v[8:9], v[164:165] op_sel_hi:[1,0] neg_lo:[0,1] neg_hi:[0,1]\n" \
  "v_pk_fma_f32 v[44:45], v[172:173], v[172:173], v[44:45]\n" \
  "v_pk_add_f32 v[172:173], v[10:11], v[164:165] op_sel_hi:[1,0] neg_lo:[0,1] neg_hi:[0,1]\n" \
  "v_pk_fma_f32 v[44:45], v[172:173], v[172:173], v[44:45]\n" \
  "v_pk_add_f32 v[172:173], v[12:13], v[164:165] op_sel_hi:[1,0] neg_lo:[0,1] neg_hi:[0,1]\n" \
  "v_pk_fma_f32 v[44:45], v[172:173], v[172:173], v[44:45]\n" \
  "v_pk_add_f32 v[172:173], v[14:15], v[164:165] op_sel_hi:[1,0] neg_lo:[0,1] neg_hi:[0,1]\n" \
  "v_pk_fma_f32 v[44:45], v[172:173], v[172:173], v[44:45]\n" \
  "v_pk_add_f32 v[172:173], v[16:17], v[164:165] op_sel_hi:[1,0] neg_lo:[0,1] neg_hi:[0,1]\n" \
  "v_pk_fma_f32 v[44:45], v[172:173], v[172:173], v[44:45]\n" \
  "v_pk_add_f32 v[172:173], v[18:19], v[164:165] op_sel_hi:[1,0] neg_lo:[0,1] neg_hi:[0,1]\n" \
  "v_pk_fma_f32 v[44:45], v[172:173], v[172:173], v[44:45]\n" \
  "v_pk_add_f32 v[172:173], v[20:21], v[164:165] op_sel_hi:[1,0] neg_lo:[0,1] neg_hi:[0,1]\n" \
  "v_pk_fma_f32 v[44:45], v[172:173], v[172:173], v[44:45]\n" \
  "v_pk_add_f32 v[172:173], v[22:23], v[164:165] op_sel_hi:[1,0] neg_lo:[0,1] neg_hi:[0,1]\n" \
  "v_pk_fma_f32 v[44:45], v[172:173], v[172:173], v[44:45]\n" \
  "v_pk_add_f32 v[172:173], v[24:25], v[164:165] op_sel_hi:[1,0] neg_lo:[0,1] neg_hi:[0,1]\n" \
  "v_pk_fma_f32 v[44:45], v[172:173], v[172:173], v[44:45]\n" \
  "v_pk_add_f32 v[172:173], v[26:27], v[164:165] op_sel_hi:[1,0] neg_lo:[0,1] neg_hi:[0,1]\n" \
  "v_pk_fma_f32 v[44:45], v[172:173], v[172:173], v[44:45]\n" \
  "v_pk_add_f32 v[172:173], v[28:29], v[164:165] op_sel_hi:[1,0] neg_lo:[0,1] neg_hi:[0,1]\n" \
  "v_pk_fma_f32 v[44:45], v[172:173], v[172:173], v[44:45]\n" \
  "v_pk_add_f32 v[172:173], v[30:31], v[164:165] op_sel_hi:[1,0] neg_lo:[0,1] neg_hi:[0,1]\n" \
  "v_pk_fma_f32 v[44:45], v[172:173], v[172:173], v[44:45]\n" \
  "v_pk_add_f32 v[172:173], v[32:33], v[170:171] op_sel_hi:[1,0] neg_lo:[0,1] neg_hi:[0,1]\n" \
  "v_pk_fma_f32 v[44:45], v[172:173], v[172:173], v[44:45]\n" \
  "v_pk_add_f32 v[172:173], v[34:35], v[170:171] op_sel_hi:[1,0] neg_lo:[0,1] neg_hi:[0,1]\n" \
  "v_pk_fma_f32 v[44:45], v[172:173], v[172:173], v[44:45]\n" \
  "v_pk_add_f32 v[172:173], v[36:37], v[170:171] op_sel_hi:[1,0] neg_lo:[0,1] neg_hi:[0,1]\n" \
  "v_pk_fma_f32 v[44:45], v[172:173], v[172:173], v[44:45]\n" \
  "v_pk_add_f32 v[172:173], v[38:39], v[170:171] op_sel_hi:[1,0] neg_lo:[0,1] neg_hi:[0,1]\n" \
  "v_pk_fma_f32 v[44:45], v[172:173], v[172:173], v[44:45]\n" \
  "v_add_f32 v166, v44, v45\n" \
  "v_mov_b32 v44, 0\n" \
  "v_mov_b32 v45, 0\n" \
  "v_mov_b32 v165, v166\n" \
  "s_nop 1\n" \
  "v_permlane16_swap_b32 v166, v165\n" \
  "v_add_f32 v166, v166, v165\n" \
  "v_mov_b32 v165, v166\n" \
  "s_nop 1\n" \
  "v_permlane32_swap_b32 v166, v165\n" \
  "v_add_f32 v166, v166, v165\n" \
  "v_mul_f32 v166, v185, v166\n" \
  "s_branch TPR%=\n" \
  "FB%=:\n" \
  "s_lshl_b32 s62, %[tb], 1\n" \
  "v_add_u32 v164, 0, v255\n" \
  "v_min_u32 v164, 249, v164\n" \
  "v_add_u32 v164, s62, v164\n" \
  "v_mul_u32_u24 v164, 64, v164\n" \
  "v_add_u32 v164, 4096, v164\n" \
  "s_mov_b32 s51, 262144\n" \
  "PF0%=:\n" \
  "buffer_load_dwordx2 v[186:187], v164, s[44:47], 0 offen sc1\n" \
  "s_waitcnt vmcnt(0)\n" \
  "v_cmp_ne_u64 vcc, s[48:49], v[186:187]\n" \
  "s_nop 0\n" \
  "s_cmp_eq_u64 vcc, 0\n" \
  "s_cbranch_scc1 PF0D%=\n" \
  "s_sub_u32 s51, s51, 1\n" \
  "s_cmp_lg_u32 s51, 0\n" \
  "s_cbranch_scc1 PF0%=\n" \
  "PF0D%=:\n" \
  "v_add_u32 v164, 64, v255\n" \
  "v_min_u32 v164, 249, v164\n" \
  "v_add_u32 v164, s62, v164\n" \
  "v_mul_u32_u24 v164, 64, v164\n" \
  "v_add_u32 v164, 4096, v164\n" \
  "s_mov_b32 s51, 262144\n" \
  "PF1%=:\n" \
  "buffer_load_dwordx2 v[186:187], v164, s[44:47], 0 offen sc1\n" \
  "s_waitcnt vmcnt(0)\n" \
  "v_cmp_ne_u64 vcc, s[48:49], v[186:187]\n" \
  "s_nop 0\n" \
  "s_cmp_eq_u64 vcc, 0\n" \
  "s_cbranch_scc1 PF1D%=\n" \
  "s_sub_u32 s51, s51, 1\n" \
  "s_cmp_lg_u32 s51, 0\n" \
  "s_cbranch_scc1 PF1%=\n" \
  "PF1D%=:\n" \
  "v_add_u32 v164, 128, v255\n" \
  "v_min_u32 v164, 249, v164\n" \
  "v_add_u32 v164, s62, v164\n" \
  "v_mul_u32_u24 v164, 64, v164\n" \
  "v_add_u32 v164, 4096, v164\n" \
  "s_mov_b32 s51, 262144\n" \
  "PF2%=:\n" \
  "buffer_load_dwordx2 v[186:187], v164, s[44:47], 0 offen sc1\n" \
  "s_waitcnt vmcnt(0)\n" \
  "v_cmp_ne_u64 vcc, s[48:49], v[186:187]\n" \
  "s_nop 0\n" \
  "s_cmp_eq_u64 vcc, 0\n" \
  "s_cbranch_scc1 PF2D%=\n" \
  "s_sub_u32 s51, s51, 1\n" \
  "s_cmp_lg_u32 s51, 0\n" \
  "s_cbranch_scc1 PF2%=\n" \
  "PF2D%=:\n" \
  "v_add_u32 v164, 192, v255\n" \
  "v_min_u32 v164, 249, v164\n" \
  "v_add_u32 v164, s62, v164\n" \
  "v_mul_u32_u24 v164, 64, v164\n" \
  "v_add_u32 v164, 4096, v164\n" \
  "s_mov_b32 s51, 262144\n" \
  "PF3%=:\n" \
  "buffer_load_dwordx2 v[186:187], v164, s[44:47], 0 offen sc1\n" \
  "s_waitcnt vmcnt(0)\n" \
  "v_cmp_ne_u64 vcc, s[48:49], v[186:187]\n" \
  "s_nop 0\n" \
  "s_cmp_eq_u64 vcc, 0\n" \
  "s_cbranch_scc1 PF3D%=\n" \
  "s_sub_u32 s51, s51, 1\n" \
  "s_cmp_lg_u32 s51, 0\n" \
  "s_cbranch_scc1 PF3%=\n" \
  "PF3D%=:\n" \
  "ds_read_b128 a[28:31], v191 offset:7168\n" \
  "ds_read_b128 a[32:35], v191 offset:8192\n" \
  "ds_read_b128 a[36:39], v191 offset:9216\n" \
  "ds_read_b128 a[40:43], v191 offset:10240\n" \
  "ds_read_b128 a[44:47], v191 offset:11264\n" \
  "ds_read_b128 a[48:51], v191 offset:12288\n" \
  "ds_read_b128 a[52:55], v191 offset:13312\n" \
  "ds_read_b128 a[56:59], v191 offset:14336\n" \
  "ds_read_b128 a[60:63], v191 offset:15360\n" \
  "ds_read_b128 a[64:67], v191 offset:16384\n" \
  "ds_read_b128 a[68:71], v191 offset:17408\n" \
  "s_waitcnt vmcnt(0) lgkmcnt(0)\n" \
  "v_subrev_u32 v164, s42, v228\n" \
  "v_mov_b32 v232, 0xff7fffff\n" \
  "v_mov_b32 v233, 0xff7fffff\n" \
  "s_add_u32 s62, %[tb], %[w]\n" \
  "s_mul_i32 s62, s62, 9216\n" \
  "s_add_u32 s62, s62, 262144\n" \
  "s_mov_b32 s63, %[w]\n" \
  "FL%=:\n" \
  "s_add_u32 s64, s62, 0x1000\n" \
  "s_add_u32 s65, s62, 0x2000\n" \
  "buffer_load_dwordx4 a[72:75], v164, s[44:47], s62 offen offset:0 sc1\n" \
  "buffer_load_dwordx4 a[76:79], v164, s[44:47], s62 offen offset:1024 sc1\n" \
  "buffer_load_dwordx4 a[80:83], v164, s[44:47], s62 offen offset:2048 sc1\n" \
  "buffer_load_dwordx4 a[84:87], v164, s[44:47], s62 offen offset:3072 sc1\n" \
  "buffer_load_dwordx4 a[88:91], v164, s[44:47], s64 offen offset:0 sc1\n" \
  "buffer_load_dwordx4 a[92:95], v164, s[44:47], s64 offen offset:1024 sc1\n" \
  "buffer_load_dwordx4 a[96:99], v164, s[44:47], s64 offen offset:2048 sc1\n" \
  "buffer_load_dwordx4 a[100:103], v164, s[44:47], s64 offen offset:3072 sc1\n" \
  "buffer_load_dwordx4 a[104:107], v164, s[44:47], s65 offen offset:0 sc1\n" \
  "s_waitcnt vmcnt(8)\n" \
  "v_mfma_f32_32x32x16_bf16 v[144:159], a[72:75], a[0:3], 0\n" \
  "v_mfma_f32_32x32x16_bf16 v[168:183], a[72:75], a[36:39], 0\n" \
  "s_waitcnt vmcnt(7)\n" \
  "v_mfma_f32_32x32x16_bf16 v[144:159], a[76:79], a[4:7], v[144:159]\n" \
  "v_mfma_f32_32x32x16_bf16 v[168:183], a[76:79], a[40:43], v[168:183]\n" \
  "s_waitcnt vmcnt(6)\n" \
  "v_mfma_f32_32x32x16_bf16 v[144:159], a[80:83], a[8:11], v[144:159]\n" \
  "v_mfma_f32_32x32x16_bf16 v[168:183], a[80:83], a[44:47], v[168:183]\n" \
  "s_waitcnt vmcnt(5)\n" \
  "v_mfma_f32_32x32x16_bf16 v[144:159], a[84:87], a[12:15], v[144:159]\n" \
  "v_mfma_f32_32x32x16_bf16 v[168:183], a[84:87], a[48:51], v[168:183]\n" \
  "s_waitcnt vmcnt(4)\n" \
  "v_mfma_f32_32x32x16_bf16 v[144:159], a[88:91], a[16:19], v[144:159]\n" \
  "v_mfma_f32_32x32x16_bf16 v[168:183], a[88:91], a[52:55], v[168:183]\n" \
  "s_waitcnt vmcnt(3)\n" \
  "v_mfma_f32_32x32x16_bf16 v[144:159], a[92:95], a[20:23], v[144:159]\n" \
  "v_mfma_f32_32x32x16_bf16 v[168:183], a[92:95], a[56:59], v[168:183]\n" \
  "s_waitcnt vmcnt(2)\n" \
  "v_mfma_f32_32x32x16_bf16 v[144:159], a[96:99], a[24:27], v[144:159]\n" \
  "v_mfma_f32_32x32x16_bf16 v[168:183], a[96:99], a[60:63], v[168:183]\n" \
  "s_waitcnt vmcnt(1)\n" \
  "v_mfma_f32_32x32x16_bf16 v[144:159], a[100:103], a[28:31], v[144:159]\n" \
  "v_mfma_f32_32x32x16_bf16 v[168:183], a[100:103], a[64:67], v[168:183]\n" \
  "s_waitcnt vmcnt(0)\n" \
  "v_mfma_f32_32x32x16_bf16 v[144:159], a[104:107], a[32:35], v[144:159]\n" \
  "v_mfma_f32_32x32x16_bf16 v[168:183], a[104:107], a[68:71], v[168:183]\n" \
  "s_nop 3\n" \
  "v_max3_f32 v232, v232, v144, v145\n" \
  "s_nop 6\n" \
  "v_max3_f32 v233, v233, v168, v169\n" \
  "v_max3_f32 v232, v232, v146, v147\n" \
  "v_max3_f32 v233, v233, v170, v171\n" \
  "v_max3_f32 v232, v232, v148, v149\n" \
  "v_max3_f32 v233, v233, v172, v173\n" \
  "v_max3_f32 v232, v232, v150, v151\n" \
  "v_max3_f32 v233, v233, v174, v175\n" \
  "v_max3_f32 v232, v232, v152, v153\n" \
  "v_max3_f32 v233, v233, v176, v177\n" \
  "v_max3_f32 v232, v232, v154, v155\n" \
  "v_max3_f32 v233, v233, v178, v179\n" \
  "v_max3_f32 v232, v232, v156, v157\n" \
  "v_max3_f32 v233, v233, v180, v181\n" \
  "v_max3_f32 v232, v232, v158, v159\n" \
  "v_max3_f32 v233, v233, v182, v183\n" \
  "s_add_u32 s62, s62, 36864\n" \
  "s_add_u32 s63, s63, 4\n" \
  "s_cmp_lt_u32 s63, 125\n" \
  "s_cbranch_scc1 FL%=\n" \
  "s_lshl_b32 s62, %[w], 9\n" \
  "s_add_u32 s62, s62, 110592\n" \
  "v_lshlrev_b32 v164, 3, v255\n" \
  "v_add_u32 v164, s62, v164\n" \
  "ds_write_b64 v164, v[232:233]\n" \
  "v_and_b32 v164, 31, v255\n" \
  "v_lshlrev_b32 v164, 3, v164\n" \
  "v_add_u32 v164, 110592, v164\n" \
  "s_waitcnt lgkmcnt(0)\n" \
  "s_barrier\n" \
  "ds_read_b64 v[144:145], v164 offset:0\n" \
  "ds_read_b64 v[146:147], v164 offset:256\n" \
  "ds_read_b64 v[148:149], v164 offset:512\n" \
  "ds_read_b64 v[150:151], v164 offset:768\n" \
  "ds_read_b64 v[152:153], v164 offset:1024\n" \
  "ds_read_b64 v[154:155], v164 offset:1280\n" \
  "ds_read_b64 v[156:157], v164 offset:1536\n" \
  "ds_read_b64 v[158:159], v164 offset:1792\n" \
  "s_waitcnt lgkmcnt(6)\n" \
  "v_max3_f32 v232, v232, v144, v146\n" \
  "v_max3_f32 v233, v233, v145, v147\n" \
  "s_waitcnt lgkmcnt(4)\n" \
  "v_max3_f32 v232, v232, v148, v150\n" \
  "v_max3_f32 v233, v233, v149, v151\n" \
  "s_waitcnt lgkmcnt(2)\n" \
  "v_max3_f32 v232, v232, v152, v154\n" \
  "v_max3_f32 v233, v233, v153, v155\n" \
  "s_waitcnt lgkmcnt(0)\n" \
  "v_max3_f32 v232, v232, v156, v158\n" \
  "v_max3_f32 v233, v233, v157, v159\n" \
  "v_sub_f32 v192, 0, v232\n" \
  "v_sub_f32 v208, 0, v233\n" \
  "v_sub_f32 v193, 0, v232\n" \
  "v_sub_f32 v209, 0, v233\n" \
  "v_sub_f32 v194, 0, v232\n" \
  "v_sub_f32 v210, 0, v233\n" \
  "v_sub_f32 v195, 0, v232\n" \
  "v_sub_f32 v211, 0, v233\n" \
  "v_sub_f32 v196, 0, v232\n" \
  "v_sub_f32 v212, 0, v233\n" \
  "v_sub_f32 v197, 0, v232\n" \
  "v_sub_f32 v213, 0, v233\n" \
  "v_sub_f32 v198, 0, v232\n" \
  "v_sub_f32 v214, 0, v233\n" \
  "v_sub_f32 v199, 0, v232\n" \
  "v_sub_f32 v215, 0, v233\n" \
  "v_sub_f32 v200, 0, v232\n" \
  "v_sub_f32 v216, 0, v233\n" \
  "v_sub_f32 v201, 0, v232\n" \
  "v_sub_f32 v217, 0, v233\n" \
  "v_sub_f32 v202, 0, v232\n" \
  "v_sub_f32 v218, 0, v233\n" \
  "v_sub_f32 v203, 0, v232\n" \
  "v_sub_f32 v219, 0, v233\n" \
  "v_sub_f32 v204, 0, v232\n" \
  "v_sub_f32 v220, 0, v233\n" \
  "v_sub_f32 v205, 0, v232\n" \
  "v_sub_f32 v221, 0, v233\n" \
  "v_sub_f32 v206, 0, v232\n" \
  "v_sub_f32 v222, 0, v233\n" \
  "v_sub_f32 v207, 0, v232\n" \
  "v_sub_f32 v223, 0, v233\n" \
  "ds_read_b128 a[72:75], v224 offset:0\n" \
  "ds_read_b128 a[76:79], v224 offset:1024\n" \
  "ds_read_b128 a[80:83], v224 offset:2048\n" \
  "ds_read_b128 a[84:87], v224 offset:3072\n" \
  "ds_read_b128 a[88:91], v224 offset:4096\n" \
  "ds_read_b128 a[92:95], v224 offset:5120\n" \
  "ds_read_b128 a[96:99], v224 offset:6144\n" \
  "s_waitcnt lgkmcnt(0)\n" \
  "s_branch FBR%=\n" \
  "EXIT%=:\n" \
  "v_cvt_pk_bf16_f32 v185, v162, v163\n" \
  "v_lshrrev_b32 v229, 1, v227\n" \
  "v_cvt_pk_bf16_f32 v189, v170, v171\n" \
  "v_add_u32 v231, s42, v227\n" \
  "v_cvt_pk_bf16_f32 v186, v164, v165\n" \
  "s_mul_i32 s62, s66, 4608\n" \
  "v_permlane16_swap_b32 v184, v188\n" \
  "v_add_u32 v230, s42, v229\n" \
  "v_cvt_pk_bf16_f32 v190, v172, v173\n" \
  "s_lshl_b32 s63, s67, 11\n" \
  "v_permlane16_swap_b32 v185, v189\n" \
  "v_add_u32 v229, s62, v229\n" \
  "v_cvt_pk_bf16_f32 v187, v166, v167\n" \
  "s_add_u32 s63, s63, s62\n" \
  "v_cvt_pk_bf16_f32 v191, v174, v175\n" \
  "v_add_u32 v224, 147456, v251\n" \
  "v_permlane16_swap_b32 v186, v190\n" \
  "v_add_u32 v228, s63, v227\n" \
  "v_permlane16_swap_b32 v187, v191\n" \
  "v_mov_b32 v234, 157264\n" \
  "s_waitcnt vmcnt(0) lgkmcnt(0)\n" \
  "v_cmp_ne_u64 vcc, s[48:49], v[232:233]\n" \
  "s_nop 0\n" \
  "s_cmp_eq_u64 vcc, 0\n" \
  "s_cbranch_scc1 PL2D%=\n" \
  "s_mov_b32 s51, 262144\n" \
  "PL2%=:\n" \
  "buffer_load_dwordx2 v[232:233], v235, s[44:47], 0 offen sc1\n" \
  "s_waitcnt vmcnt(0)\n" \
  "v_cmp_ne_u64 vcc, s[48:49], v[232:233]\n" \
  "s_nop 0\n" \
  "s_cmp_eq_u64 vcc, 0\n" \
  "s_cbranch_scc1 PL2D%=\n" \
  "s_sub_u32 s51, s51, 1\n" \
  "s_cmp_lg_u32 s51, 0\n" \
  "s_cbranch_scc1 PL2%=\n" \
  "PL2D%=:\n" \
  "v_mfma_f32_16x16x32_bf16 v[8:11], a[108:111], v[184:187], v[8:11]\n" \
  "buffer_load_dwordx4 a[0:3], v227, s[44:47], s58 offen offset:0 sc1\n" \
  "v_cvt_pk_bf16_f32 v192, v0, v1\n" \
  "v_cvt_pk_bf16_f32 v193, v2, v3\n" \
  "v_mfma_f32_16x16x32_bf16 v[12:15], a[108:111], v[188:191], v[12:15]\n" \
  "v_cvt_pk_bf16_f32 v194, v16, v17\n" \
  "v_cvt_pk_bf16_f32 v195, v18, v19\n" \
  "v_mfma_f32_16x16x32_bf16 v[24:27], a[112:115], v[184:187], v[24:27]\n" \
  "ds_write_b128 v231, v[192:195] offset:0\n" \
  "v_cvt_pk_bf16_f32 v196, v32, v33\n" \
  "v_cvt_pk_bf16_f32 v197, v34, v35\n" \
  "v_mfma_f32_16x16x32_bf16 v[28:31], a[112:115], v[188:191], v[28:31]\n" \
  "buffer_load_dwordx4 a[4:7], v227, s[44:47], s58 offen offset:1024 sc1\n" \
  "v_cvt_pk_bf16_f32 v198, v48, v49\n" \
  "v_cvt_pk_bf16_f32 v199, v50, v51\n" \
  "v_mfma_f32_16x16x32_bf16 v[40:43], a[116:119], v[184:187], v[40:43]\n" \
  "ds_write_b128 v231, v[196:199] offset:1024\n" \
  "v_cvt_pk_bf16_f32 v200, v64, v65\n" \
  "v_cvt_pk_bf16_f32 v201, v66, v67\n" \
  "v_mfma_f32_16x16x32_bf16 v[44:47], a[116:119], v[188:191], v[44:47]\n" \
  "v_cvt_pk_bf16_f32 v202, v80, v81\n" \
  "v_cvt_pk_bf16_f32 v203, v82, v83\n" \
  "v_mfma_f32_16x16x32_bf16 v[56:59], a[120:123], v[184:187], v[56:59]\n" \
  "buffer_load_dwordx4 a[8:11], v227, s[44:47], s58 offen offset:2048 sc1\n" \
  "ds_write_b128 v231, v[200:203] offset:2048\n" \
  "v_cvt_pk_bf16_f32 v204, v96, v97\n" \
  "v_cvt_pk_bf16_f32 v205, v98, v99\n" \
  "v_mfma_f32_16x16x32_bf16 v[60:63], a[120:123], v[188:191], v[60:63]\n" \
  "v_cvt_pk_bf16_f32 v206, v112, v113\n" \
  "v_cvt_pk_bf16_f32 v207, v114, v115\n" \
  "v_mfma_f32_16x16x32_bf16 v[72:75], a[124:127], v[184:187], v[72:75]\n" \
  "ds_write_b128 v231, v[204:207] offset:3072\n" \
  "v_cvt_pk_bf16_f32 v160, v128, v129\n" \
  "v_cvt_pk_bf16_f32 v161, v130, v131\n" \
  "v_mfma_f32_16x16x32_bf16 v[76:79], a[124:127], v[188:191], v[76:79]\n" \
  "buffer_load_dwordx4 a[12:15], v227, s[44:47], s58 offen offset:3072 sc1\n" \
  "ds_write_b64 v230, v[160:161] offset:4096\n" \
  "v_cvt_pk_bf16_f32 v208, v4, v5\n" \
  "v_cvt_pk_bf16_f32 v209, v6, v7\n" \
  "v_mfma_f32_16x16x32_bf16 v[88:91], a[128:131], v[184:187], v[88:91]\n" \
  "v_cvt_pk_bf16_f32 v210, v20, v21\n" \
  "v_cvt_pk_bf16_f32 v211, v22, v23\n" \
  "v_mfma_f32_16x16x32_bf16 v[92:95], a[128:131], v[188:191], v[92:95]\n" \
  "ds_write_b128 v231, v[208:211] offset:4608\n" \
  "v_cvt_pk_bf16_f32 v212, v36, v37\n" \
  "v_cvt_pk_bf16_f32 v213, v38, v39\n" \
  "v_mfma_f32_16x16x32_bf16 v[104:107], a[132:135], v[184:187], v[104:107]\n" \
  "s_add_u32 s58, s58, 0x1000\n" \
  "buffer_load_dwordx4 a[16:19], v227, s[44:47], s58 offen offset:0 sc1\n" \
  "v_cvt_pk_bf16_f32 v214, v52, v53\n" \
  "v_cvt_pk_bf16_f32 v215, v54, v55\n" \
  "v_mfma_f32_16x16x32_bf16 v[108:111], a[132:135], v[188:191], v[108:111]\n" \
  "ds_write_b128 v231, v[212:215] offset:5632\n" \
  "v_cvt_pk_bf16_f32 v216, v68, v69\n" \
  "v_cvt_pk_bf16_f32 v217, v70, v71\n" \
  "v_mfma_f32_16x16x32_bf16 v[120:123], a[136:139], v[184:187], v[120:123]\n" \
  "v_cvt_pk_bf16_f32 v218, v84, v85\n" \
  "v_cvt_pk_bf16_f32 v219, v86, v87\n" \
  "v_mfma_f32_16x16x32_bf16 v[124:127], a[136:139], v[188:191], v[124:127]\n" \
  "buffer_load_dwordx4 a[20:23], v227, s[44:47], s58 offen offset:1024 sc1\n" \
  "ds_write_b128 v231, v[216:219] offset:6656\n" \
  "v_cvt_pk_bf16_f32 v220, v100, v101\n" \
  "v_cvt_pk_bf16_f32 v221, v102, v103\n" \
  "v_mfma_f32_16x16x32_bf16 v[136:139], a[140:143], v[184:187], v[136:139]\n" \
  "v_cvt_pk_bf16_f32 v222, v116, v117\n" \
  "v_cvt_pk_bf16_f32 v223, v118, v119\n" \
  "v_mfma_f32_16x16x32_bf16 v[140:143], a[140:143], v[188:191], v[140:143]\n" \
  "ds_write_b128 v231, v[220:223] offset:7680\n" \
  "v_cvt_pk_bf16_f32 v162, v132, v133\n" \
  "v_cvt_pk_bf16_f32 v163, v134, v135\n" \
  "ds_write_b64 v230, v[162:163] offset:8704\n" \
  "v_cmp_gt_u32 vcc, 2, v253\n" \
  "v_add_u32 v164, 224, v224\n" \
  "v_add_u32 v165, 4832, v224\n" \
  "v_add_u32 v166, 9440, v224\n" \
  "v_add_u32 v167, 544, v224\n" \
  "v_add_u32 v168, 5152, v224\n" \
  "v_cndmask_b32 v164, v164, v234, vcc\n" \
  "v_cndmask_b32 v165, v165, v234, vcc\n" \
  "v_cndmask_b32 v166, v166, v234, vcc\n" \
  "v_cndmask_b32 v167, v234, v167, vcc\n" \
  "v_cndmask_b32 v168, v234, v168, vcc\n" \
  "s_cmp_lg_u32 %[w], 3\n" \
  "s_cbranch_scc1 NE8%=\n" \
  "ds_read_b128 v[0:3], v224 offset:0\n" \
  "ds_read_b128 v[4:7], v224 offset:64\n" \
  "ds_read_b128 v[16:19], v224 offset:128\n" \
  "ds_read_b128 v[20:23], v224 offset:192\n" \
  "ds_read_b128 v[32:35], v164\n" \
  "ds_read_b128 v[36:39], v224 offset:4608\n" \
  "ds_read_b128 v[48:51], v224 offset:4672\n" \
  "ds_read_b128 v[52:55], v224 offset:4736\n" \
  "ds_read_b128 v[64:67], v224 offset:4800\n" \
  "ds_read_b128 v[68:71], v165\n" \
  "ds_read_b128 v[80:83], v224 offset:9216\n" \
  "ds_read_b128 v[84:87], v224 offset:9280\n" \
  "ds_read_b128 v[96:99], v224 offset:9344\n" \
  "ds_read_b128 v[100:103], v224 offset:9408\n" \
  "ds_read_b128 v[112:115], v166\n" \
  "v_mov_b32 v172, -0.10294323970836089\n" \
  "v_mov_b32 v170, -2.302208198144325\n" \
  "v_add_u32 v174, 101376, v227\n" \
  "v_lshl_add_u32 v175, v253, 4, v254\n" \
  "v_mul_u32_u24 v175, 48, v175\n" \
  "v_add_u32 v175, 157344, v175\n" \
  "s_waitcnt lgkmcnt(14)\n" \
  "v_mfma_f32_16x16x32_bf16 v[116:119], a[232:235], v[0:3], 0\n" \
  "s_waitcnt lgkmcnt(13)\n" \
  "v_mfma_f32_16x16x32_bf16 v[116:119], a[236:239], v[4:7], v[116:119]\n" \
  "s_waitcnt lgkmcnt(12)\n" \
  "v_mfma_f32_16x16x32_bf16 v[116:119], a[240:243], v[16:19], v[116:119]\n" \
  "s_waitcnt lgkmcnt(11)\n" \
  "v_mfma_f32_16x16x32_bf16 v[116:119], a[244:247], v[20:23], v[116:119]\n" \
  "s_waitcnt lgkmcnt(7)\n" \
  "v_mfma_f32_16x16x32_bf16 v[116:119], a[248:251], v[32:35], v[116:119]\n" \
  "ds_read_b128 v[0:3], v224 offset:288\n" \
  "ds_read_b128 v[4:7], v224 offset:352\n" \
  "ds_read_b128 v[16:19], v224 offset:416\n" \
  "ds_read_b128 v[20:23], v224 offset:480\n" \
  "ds_read_b128 v[32:35], v167\n" \
  "s_nop 2\n" \
  "v_pk_mul_f32 v[144:145], v[116:117], v[116:117]\n" \
  "v_pk_mul_f32 v[146:147], v[118:119], v[118:119]\n" \
  "v_pk_fma_f32 v[144:145], v[144:145], v[172:173], v[170:171] op_sel_hi:[1,0,0]\n" \
  "v_pk_fma_f32 v[146:147], v[146:147], v[172:173], v[170:171] op_sel_hi:[1,0,0]\n" \
  "v_pk_mul_f32 v[144:145], v[144:145], v[116:117]\n" \
  "v_pk_mul_f32 v[146:147], v[146:147], v[118:119]\n" \
  "v_mfma_f32_16x16x32_bf16 v[128:131], a[232:235], v[36:39], 0\n" \
  "v_exp_f32 v144, v144\n" \
  "v_mfma_f32_16x16x32_bf16 v[128:131], a[236:239], v[48:51], v[128:131]\n" \
  "v_exp_f32 v145, v145\n" \
  "v_mfma_f32_16x16x32_bf16 v[128:131], a[240:243], v[52:55], v[128:131]\n" \
  "v_exp_f32 v146, v146\n" \
  "s_waitcnt lgkmcnt(5)\n" \
  "v_mfma_f32_16x16x32_bf16 v[128:131], a[244:247], v[64:67], v[128:131]\n" \
  "v_exp_f32 v147, v147\n" \
  "v_pk_add_f32 v[144:145], v[144:145], 1.0 op_sel_hi:[1,0]\n" \
  "v_pk_add_f32 v[146:147], v[146:147], 1.0 op_sel_hi:[1,0]\n" \
  "v_mfma_f32_16x16x32_bf16 v[128:131], a[248:251], v[68:71], v[128:131]\n" \
  "v_rcp_f32 v144, v144\n" \
  "v_rcp_f32 v145, v145\n" \
  "v_rcp_f32 v146, v146\n" \
  "v_rcp_f32 v147, v147\n" \
  "v_pk_mul_f32 v[116:117], v[116:117], v[144:145]\n" \
  "v_pk_mul_f32 v[118:119], v[118:119], v[146:147]\n" \
  "v_pk_add_f32 v[148:149], v[116:117], v[118:119]\n" \
  "v_pk_mul_f32 v[150:151], v[116:117], v[116:117]\n" \
  "v_pk_fma_f32 v[150:151], v[118:119], v[118:119], v[150:151]\n" \
  "ds_write_b128 v174, v[116:119] offset:0\n" \
  "ds_read_b128 v[36:39], v224 offset:4896\n" \
  "ds_read_b128 v[48:51], v224 offset:4960\n" \
  "ds_read_b128 v[52:55], v224 offset:5024\n" \
  "ds_read_b128 v[64:67], v224 offset:5088\n" \
  "ds_read_b128 v[68:71], v168\n" \
  "v_pk_mul_f32 v[144:145], v[128:129], v[128:129]\n" \
  "v_pk_mul_f32 v[146:147], v[130:131], v[130:131]\n" \
  "v_pk_fma_f32 v[144:145], v[144:145], v[172:173], v[170:171] op_sel_hi:[1,0,0]\n" \
  "v_pk_fma_f32 v[146:147], v[146:147], v[172:173], v[170:171] op_sel_hi:[1,0,0]\n" \
  "v_pk_mul_f32 v[144:145], v[144:145], v[128:129]\n" \
  "v_pk_mul_f32 v[146:147], v[146:147], v[130:131]\n" \
  "v_mfma_f32_16x16x32_bf16 v[116:119], a[232:235], v[80:83], 0\n" \
  "v_exp_f32 v144, v144\n" \
  "v_mfma_f32_16x16x32_bf16 v[116:119], a[236:239], v[84:87], v[116:119]\n" \
  "v_exp_f32 v145, v145\n" \
  "v_mfma_f32_16x16x32_bf16 v[116:119], a[240:243], v[96:99], v[116:119]\n" \
  "v_exp_f32 v146, v146\n" \
  "v_mfma_f32_16x16x32_bf16 v[116:119], a[244:247], v[100:103], v[116:119]\n" \
  "v_exp_f32 v147, v147\n" \
  "v_pk_add_f32 v[144:145], v[144:145], 1.0 op_sel_hi:[1,0]\n" \
  "v_pk_add_f32 v[146:147], v[146:147], 1.0 op_sel_hi:[1,0]\n" \
  "v_mfma_f32_16x16x32_bf16 v[116:119], a[248:251], v[112:115], v[116:119]\n" \
  "v_rcp_f32 v144, v144\n" \
  "v_rcp_f32 v145, v145\n" \
  "v_rcp_f32 v146, v146\n" \
  "v_rcp_f32 v147, v147\n" \
  "v_pk_mul_f32 v[128:129], v[128:129], v[144:145]\n" \
  "v_pk_mul_f32 v[130:131], v[130:131], v[146:147]\n" \
  "v_pk_add_f32 v[152:153], v[128:129], v[130:131]\n" \
  "v_pk_mul_f32 v[154:155], v[128:129], v[128:129]\n" \
  "v_pk_fma_f32 v[154:155], v[130:131], v[130:131], v[154:155]\n" \
  "ds_write_b128 v174, v[128:131] offset:1024\n" \
  "v_pk_mul_f32 v[144:145], v[116:117], v[116:117]\n" \
  "v_pk_mul_f32 v[146:147], v[118:119], v[118:119]\n" \
  "v_pk_fma_f32 v[144:145], v[144:145], v[172:173], v[170:171] op_sel_hi:[1,0,0]\n" \
  "v_pk_fma_f32 v[146:147], v[146:147], v[172:173], v[170:171] op_sel_hi:[1,0,0]\n" \
  "v_pk_mul_f32 v[144:145], v[144:145], v[116:117]\n" \
  "v_pk_mul_f32 v[146:147], v[146:147], v[118:119]\n" \
  "s_waitcnt lgkmcnt(1)\n" \
  "v_mfma_f32_16x16x32_bf16 v[128:131], a[216:219], v[0:3], 0\n" \
  "v_exp_f32 v144, v144\n" \
  "v_mfma_f32_16x16x32_bf16 v[128:131], a[220:223], v[4:7], v[128:131]\n" \
  "v_exp_f32 v145, v145\n" \
  "v_mfma_f32_16x16x32_bf16 v[128:131], a[224:227], v[16:19], v[128:131]\n" \
  "v_exp_f32 v146, v146\n" \
  "v_mfma_f32_16x16x32_bf16 v[128:131], a[228:231], v[20:23], v[128:131]\n" \
  "v_exp_f32 v147, v147\n" \
  "v_pk_add_f32 v[144:145], v[144:145], 1.0 op_sel_hi:[1,0]\n" \
  "v_pk_add_f32 v[146:147], v[146:147], 1.0 op_sel_hi:[1,0]\n" \
  "v_mfma_f32_16x16x32_bf16 v[128:131], a[248:251], v[32:35], v[128:131]\n" \
  "v_rcp_f32 v144, v144\n" \
  "v_rcp_f32 v145, v145\n" \
  "v_rcp_f32 v146, v146\n" \
  "v_rcp_f32 v147, v147\n" \
  "v_pk_mul_f32 v[116:117], v[116:117], v[144:145]\n" \
  "v_pk_mul_f32 v[118:119], v[118:119], v[146:147]\n" \
  "v_pk_add_f32 v[156:157], v[116:117], v[118:119]\n" \
  "v_pk_mul_f32 v[158:159], v[116:117], v[116:117]\n" \
  "v_pk_fma_f32 v[158:159], v[118:119], v[118:119], v[158:159]\n" \
  "ds_write_b128 v174, v[116:119] offset:2048\n" \
  "v_pk_mul_f32 v[144:145], v[128:129], v[128:129]\n" \
  "v_pk_mul_f32 v[146:147], v[130:131], v[130:131]\n" \
  "v_pk_fma_f32 v[144:145], v[144:145], v[172:173], v[170:171] op_sel_hi:[1,0,0]\n" \
  "v_pk_fma_f32 v[146:147], v[146:147], v[172:173], v[170:171] op_sel_hi:[1,0,0]\n" \
  "v_pk_mul_f32 v[144:145], v[144:145], v[128:129]\n" \
  "v_pk_mul_f32 v[146:147], v[146:147], v[130:131]\n" \
  "v_mfma_f32_16x16x32_bf16 v[116:119], a[216:219], v[36:39], 0\n" \
  "v_exp_f32 v144, v144\n" \
  "v_mfma_f32_16x16x32_bf16 v[116:119], a[220:223], v[48:51], v[116:119]\n" \
  "v_exp_f32 v145, v145\n" \
  "v_mfma_f32_16x16x32_bf16 v[116:119], a[224:227], v[52:55], v[116:119]\n" \
  "v_exp_f32 v146, v146\n" \
  "v_mfma_f32_16x16x32_bf16 v[116:119], a[228:231], v[64:67], v[116:119]\n" \
  "v_exp_f32 v147, v147\n" \
  "v_pk_add_f32 v[144:145], v[144:145], 1.0 op_sel_hi:[1,0]\n" \
  "v_pk_add_f32 v[146:147], v[146:147], 1.0 op_sel_hi:[1,0]\n" \
  "v_mfma_f32_16x16x32_bf16 v[116:119], a[248:251], v[68:71], v[116:119]\n" \
  "v_rcp_f32 v144, v144\n" \
  "v_rcp_f32 v145, v145\n" \
  "v_rcp_f32 v146, v146\n" \
  "v_rcp_f32 v147, v147\n" \
  "v_pk_mul_f32 v[128:129], v[128:129], v[144:145]\n" \
  "v_pk_mul_f32 v[130:131], v[130:131], v[146:147]\n" \
  "ds_write_b128 v174, v[128:131] offset:4096\n" \
  "s_nop 0\n" \
  "v_pk_mul_f32 v[144:145], v[116:117], v[116:117]\n" \
  "v_pk_mul_f32 v[146:147], v[118:119], v[118:119]\n" \
  "v_pk_fma_f32 v[144:145], v[144:145], v[172:173], v[170:171] op_sel_hi:[1,0,0]\n" \
  "v_pk_fma_f32 v[146:147], v[146:147], v[172:173], v[170:171] op_sel_hi:[1,0,0]\n" \
  "v_pk_mul_f32 v[144:145], v[144:145], v[116:117]\n" \
  "v_pk_mul_f32 v[146:147], v[146:147], v[118:119]\n" \
  "v_exp_f32 v144, v144\n" \
  "v_exp_f32 v145, v145\n" \
  "v_exp_f32 v146, v146\n" \
  "v_exp_f32 v147, v147\n" \
  "v_pk_add_f32 v[144:145], v[144:145], 1.0 op_sel_hi:[1,0]\n" \
  "v_pk_add_f32 v[146:147], v[146:147], 1.0 op_sel_hi:[1,0]\n" \
  "v_rcp_f32 v144, v144\n" \
  "v_rcp_f32 v145, v145\n" \
  "v_rcp_f32 v146, v146\n" \
  "v_rcp_f32 v147, v147\n" \
  "v_pk_mul_f32 v[116:117], v[116:117], v[144:145]\n" \
  "v_pk_mul_f32 v[118:119], v[118:119], v[146:147]\n" \
  "ds_write_b128 v174, v[116:119] offset:3072\n" \
  "v_add_f32 v148, v148, v149\n" \
  "v_add_f32 v150, v150, v151\n" \
  "v_add_f32 v152, v152, v153\n" \
  "v_add_f32 v154, v154, v155\n" \
  "v_add_f32 v156, v156, v157\n" \
  "v_add_f32 v158, v158, v159\n" \
  "s_nop 0\n" \
  "v_permlane32_swap_b32 v148, v156\n" \
  "v_permlane32_swap_b32 v152, v132\n" \
  "v_permlane32_swap_b32 v150, v158\n" \
  "v_permlane32_swap_b32 v154, v134\n" \
  "v_add_f32 v148, v148, v156\n" \
  "v_add_f32 v152, v152, v132\n" \
  "v_add_f32 v150, v150, v158\n" \
  "v_add_f32 v154, v154, v134\n" \
  "s_nop 0\n" \
  "v_permlane16_swap_b32 v148, v152\n" \
  "v_permlane16_swap_b32 v150, v154\n" \
  "s_nop 0\n" \
  "v_add_f32 v148, v148, v152\n" \
  "v_add_f32 v149, v150, v154\n" \
  "ds_write_b64 v175, v[148:149]\n" \
  "NE8%=:\n" \
  "s_waitcnt lgkmcnt(0)\n" \
  "s_barrier\n" \
  "ds_read_b128 a[108:111], v228 offset:0\n" \
  "ds_read_b128 a[112:115], v228 offset:1024\n" \
  "ds_read_b64 a[140:141], v229 offset:4096\n" \
  "ds_read_b128 a[116:119], v228 offset:9216\n" \
  "ds_read_b128 a[120:123], v228 offset:10240\n" \
  "ds_read_b64 a[142:143], v229 offset:13312\n" \
  "ds_read_b128 a[124:127], v228 offset:18432\n" \
  "ds_read_b128 a[128:131], v228 offset:19456\n" \
  "ds_read_b64 a[252:253], v229 offset:22528\n" \
  "ds_read_b128 a[132:135], v228 offset:27648\n" \
  "ds_read_b128 a[136:139], v228 offset:28672\n" \
  "ds_read_b64 a[254:255], v229 offset:31744\n" \
  "v_add_u32 v228, 36864, v228\n" \
  "v_add_u32 v229, 36864, v229\n" \
  "v_mov_b32 v154, 0\n" \
  "v_mov_b32 v155, 0\n" \
  "s_waitcnt lgkmcnt(11)\n" \
  "v_mfma_f32_16x16x32_bf16 v[172:175], v[238:241], a[108:111], 0\n" \
  "v_cvt_pk_bf16_f32 v192, v8, v9\n" \
  "v_cvt_pk_bf16_f32 v193, v10, v11\n" \
  "v_mfma_f32_16x16x32_bf16 v[176:179], v[240:243], a[108:111], 0\n" \
  "buffer_load_dwordx4 a[24:27], v227, s[44:47], s58 offen offset:2048 sc1\n" \
  "v_cvt_pk_bf16_f32 v194, v24, v25\n" \
  "v_cvt_pk_bf16_f32 v195, v26, v27\n" \
  "s_waitcnt lgkmcnt(10)\n" \
  "v_mfma_f32_16x16x32_bf16 v[180:183], v[238:241], a[112:115], 0\n" \
  "ds_write_b128 v231, v[192:195] offset:36864\n" \
  "v_cvt_pk_bf16_f32 v196, v40, v41\n" \
  "v_cvt_pk_bf16_f32 v197, v42, v43\n" \
  "v_mfma_f32_16x16x32_bf16 v[184:187], v[240:243], a[112:115], 0\n" \
  "v_cvt_pk_bf16_f32 v198, v56, v57\n" \
  "v_cvt_pk_bf16_f32 v199, v58, v59\n" \
  "s_waitcnt lgkmcnt(7)\n" \
  "v_mfma_f32_16x16x32_bf16 v[172:175], v[238:241], a[116:119], v[172:175]\n" \
  "ds_write_b128 v231, v[196:199] offset:37888\n" \
  "v_cvt_pk_bf16_f32 v200, v72, v73\n" \
  "v_cvt_pk_bf16_f32 v201, v74, v75\n" \
  "v_mfma_f32_16x16x32_bf16 v[176:179], v[240:243], a[116:119], v[176:179]\n" \
  "buffer_load_dwordx4 a[28:31], v227, s[44:47], s58 offen offset:3072 sc1\n" \
  "v_cvt_pk_bf16_f32 v202, v88, v89\n" \
  "v_cvt_pk_bf16_f32 v203, v90, v91\n" \
  "v_mfma_f32_16x16x32_bf16 v[180:183], v[238:241], a[120:123], v[180:183]\n" \
  "ds_write_b128 v231, v[200:203] offset:38912\n" \
  "v_cvt_pk_bf16_f32 v204, v104, v105\n" \
  "v_cvt_pk_bf16_f32 v205, v106, v107\n" \
  "v_mfma_f32_16x16x32_bf16 v[184:187], v[240:243], a[120:123], v[184:187]\n" \
  "v_cvt_pk_bf16_f32 v206, v120, v121\n" \
  "v_cvt_pk_bf16_f32 v207, v122, v123\n" \
  "v_mfma_f32_16x16x32_bf16 v[188:191], v[236:239], a[140:143], 0\n" \
  "ds_write_b128 v231, v[204:207] offset:39936\n" \
  "v_cvt_pk_bf16_f32 v160, v136, v137\n" \
  "v_cvt_pk_bf16_f32 v161, v138, v139\n" \
  "s_waitcnt lgkmcnt(4)\n" \
  "v_mfma_f32_16x16x32_bf16 v[172:175], v[238:241], a[124:127], v[172:175]\n" \
  "s_add_u32 s58, s58, 0x1000\n" \
  "buffer_load_dwordx4 a[32:35], v227, s[44:47], s58 offen offset:0 sc1\n" \
  "ds_write_b64 v230, v[160:161] offset:40960\n" \
  "v_cvt_pk_bf16_f32 v208, v12, v13\n" \
  "v_cvt_pk_bf16_f32 v209, v14, v15\n" \
  "v_mfma_f32_16x16x32_bf16 v[176:179], v[240:243], a[124:127], v[176:179]\n" \
  "v_cvt_pk_bf16_f32 v210, v28, v29\n" \
  "v_cvt_pk_bf16_f32 v211, v30, v31\n" \
  "v_mfma_f32_16x16x32_bf16 v[180:183], v[238:241], a[128:131], v[180:183]\n" \
  "ds_write_b128 v231, v[208:211] offset:41472\n" \
  "v_cvt_pk_bf16_f32 v212, v44, v45\n" \
  "v_cvt_pk_bf16_f32 v213, v46, v47\n" \
  "v_mfma_f32_16x16x32_bf16 v[184:187], v[240:243], a[128:131], v[184:187]\n" \
  "v_cvt_pk_bf16_f32 v214, v60, v61\n" \
  "v_cvt_pk_bf16_f32 v215, v62, v63\n" \
  "v_mfma_f32_16x16x32_bf16 v[172:175], v[238:241], a[132:135], v[172:175]\n" \
  "buffer_load_dwordx4 a[36:39], v227, s[44:47], s58 offen offset:1024 sc1\n" \
  "ds_write_b128 v231, v[212:215] offset:42496\n" \
  "v_cvt_pk_bf16_f32 v216, v76, v77\n" \
  "v_cvt_pk_bf16_f32 v217, v78, v79\n" \
  "v_mfma_f32_16x16x32_bf16 v[176:179], v[240:243], a[132:135], v[176:179]\n" \
  "v_cvt_pk_bf16_f32 v218, v92, v93\n" \
  "v_cvt_pk_bf16_f32 v219, v94, v95\n" \
  "v_mfma_f32_16x16x32_bf16 v[180:183], v[238:241], a[136:139], v[180:183]\n" \
  "ds_write_b128 v231, v[216:219] offset:43520\n" \
  "v_cvt_pk_bf16_f32 v220, v108, v109\n" \
  "v_cvt_pk_bf16_f32 v221, v110, v111\n" \
  "v_mfma_f32_16x16x32_bf16 v[184:187], v[240:243], a[136:139], v[184:187]\n" \
  "v_cvt_pk_bf16_f32 v222, v124, v125\n" \
  "v_cvt_pk_bf16_f32 v223, v126, v127\n" \
  "v_mfma_f32_16x16x32_bf16 v[188:191], v[236:239], a[252:255], v[188:191]\n" \
  "buffer_load_dwordx4 a[40:43], v227, s[44:47], s58 offen offset:2048 sc1\n" \
  "ds_write_b128 v231, v[220:223] offset:44544\n" \
  "v_cvt_pk_bf16_f32 v162, v140, v141\n" \
  "v_cvt_pk_bf16_f32 v163, v142, v143\n" \
  "ds_write_b64 v230, v[162:163] offset:45568\n" \
  "ds_read_b128 v[40:43], v224 offset:0\n" \
  "ds_read_b128 v[44:47], v224 offset:64\n" \
  "ds_read_b128 v[48:51], v224 offset:128\n" \
  "ds_read_b128 v[52:55], v224 offset:192\n" \
  "ds_read_b128 v[56:59], v164\n" \
  "ds_read_b128 v[60:63], v224 offset:4608\n" \
  "ds_read_b128 v[64:67], v224 offset:4672\n" \
  "ds_read_b128 v[68:71], v224 offset:4736\n" \
  "ds_read_b128 v[72:75], v224 offset:4800\n" \
  "ds_read_b128 v[76:79], v165\n" \
  "v_cvt_pk_bf16_f32 v144, v172, v173\n" \
  "v_cvt_pk_bf16_f32 v145, v174, v175\n" \
  "v_cvt_pk_bf16_f32 v146, v176, v177\n" \
  "v_cvt_pk_bf16_f32 v147, v178, v179\n" \
  "v_cvt_pk_bf16_f32 v148, v180, v181\n" \
  "v_cvt_pk_bf16_f32 v149, v182, v183\n" \
  "v_cvt_pk_bf16_f32 v150, v184, v185\n" \
  "v_cvt_pk_bf16_f32 v151, v186, v187\n" \
  "v_cvt_pk_bf16_f32 v152, v188, v189\n" \
  "v_cvt_pk_bf16_f32 v153, v190, v191\n" \
  "v_min_u32 v144, -2, v144\n" \
  "v_min_u32 v145, -2, v145\n" \
  "v_min_u32 v146, -2, v146\n" \
  "v_min_u32 v147, -2, v147\n" \
  "v_min_u32 v148, -2, v148\n" \
  "v_min_u32 v149, -2, v149\n" \
  "v_min_u32 v150, -2, v150\n" \
  "v_min_u32 v151, -2, v151\n" \
  "v_min_u32 v152, -2, v152\n" \
  "v_min_u32 v153, -2, v153\n" \
  "buffer_store_dwordx4 v[144:147], v227, s[44:47], s70 offen sc1\n" \
  "buffer_store_dwordx4 v[148:151], v227, s[44:47], s70 offen offset:1024 sc1\n" \
  "buffer_store_dwordx4 v[152:155], v227, s[44:47], s71 offen sc1\n" \
  "s_cmp_ge_u32 %[to], 125\n" \
  "s_cbranch_scc1 END%=\n" \
  "s_waitcnt lgkmcnt(9)\n" \
  "v_mfma_f32_16x16x32_bf16 v[120:123], a[160:163], v[40:43], 0\n" \
  "ds_read_b128 v[80:83], v224 offset:9216\n" \
  "ds_read_b128 v[84:87], v224 offset:9280\n" \
  "s_waitcnt lgkmcnt(10)\n" \
  "v_mfma_f32_16x16x32_bf16 v[120:123], a[164:167], v[44:47], v[120:123]\n" \
  "ds_read_b128 v[88:91], v224 offset:9344\n" \
  "ds_read_b128 v[92:95], v224 offset:9408\n" \
  "s_waitcnt lgkmcnt(10)\n" \
  "v_mfma_f32_16x16x32_bf16 v[120:123], a[168:171], v[48:51], v[120:123]\n" \
  "ds_read_b128 v[96:99], v166\n" \
  "ds_read_b128 v[0:3], v224 offset:288\n" \
  "v_mfma_f32_16x16x32_bf16 v[120:123], a[172:175], v[52:55], v[120:123]\n" \
  "ds_read_b128 v[4:7], v224 offset:352\n" \
  "ds_read_b128 v[8:11], v224 offset:416\n" \
  "s_waitcnt lgkmcnt(8)\n" \
  "v_mfma_f32_16x16x32_bf16 v[120:123], a[176:179], v[56:59], v[120:123]\n" \
  "ds_read_b128 v[12:15], v224 offset:480\n" \
  "ds_read_b128 v[16:19], v167\n" \
  "v_mov_b32 v172, -0.10294323970836089\n" \
  "v_mov_b32 v170, -2.302208198144325\n" \
  "v_lshl_add_u32 v174, v253, 4, v254\n" \
  "s_lshl_b32 s62, %[w], 3\n" \
  "v_mul_u32_u24 v174, 48, v174\n" \
  "v_mul_u32_u24 v226, 48, v254\n" \
  "s_add_u32 s62, s62, 157312\n" \
  "v_pk_mul_f32 v[192:193], v[120:121], v[120:121]\n" \
  "v_pk_mul_f32 v[194:195], v[122:123], v[122:123]\n" \
  "v_pk_fma_f32 v[192:193], v[192:193], v[172:173], v[170:171] op_sel_hi:[1,0,0]\n" \
  "v_pk_fma_f32 v[194:195], v[194:195], v[172:173], v[170:171] op_sel_hi:[1,0,0]\n" \
  "v_pk_mul_f32 v[192:193], v[192:193], v[120:121]\n" \
  "v_pk_mul_f32 v[194:195], v[194:195], v[122:123]\n" \
  "v_mfma_f32_16x16x32_bf16 v[124:127], a[160:163], v[60:63], 0\n" \
  "buffer_load_dwordx4 a[44:47], v227, s[44:47], s58 offen offset:3072 sc1\n" \
  "v_exp_f32 v192, v192\n" \
  "v_mfma_f32_16x16x32_bf16 v[124:127], a[164:167], v[64:67], v[124:127]\n" \
  "ds_read_b128 v[20:23], v224 offset:4896\n" \
  "v_exp_f32 v193, v193\n" \
  "v_mfma_f32_16x16x32_bf16 v[124:127], a[168:171], v[68:71], v[124:127]\n" \
  "v_exp_f32 v194, v194\n" \
  "v_mfma_f32_16x16x32_bf16 v[124:127], a[172:175], v[72:75], v[124:127]\n" \
  "ds_read_b128 v[24:27], v224 offset:4960\n" \
  "v_exp_f32 v195, v195\n" \
  "v_pk_add_f32 v[192:193], v[192:193], 1.0 op_sel_hi:[1,0]\n" \
  "v_pk_add_f32 v[194:195], v[194:195], 1.0 op_sel_hi:[1,0]\n" \
  "v_mfma_f32_16x16x32_bf16 v[124:127], a[176:179], v[76:79], v[124:127]\n" \
  "s_add_u32 s58, s58, 0x1000\n" \
  "buffer_load_dwordx4 a[48:51], v227, s[44:47], s58 offen offset:0 sc1\n" \
  "v_rcp_f32 v192, v192\n" \
  "v_rcp_f32 v193, v193\n" \
  "v_rcp_f32 v194, v194\n" \
  "v_rcp_f32 v195, v195\n" \
  "v_pk_mul_f32 v[120:121], v[120:121], v[192:193]\n" \
  "v_pk_mul_f32 v[122:123], v[122:123], v[194:195]\n" \
  "v_pk_add_f32 v[196:197], v[120:121], v[122:123]\n" \
  "v_pk_mul_f32 v[198:199], v[120:121], v[120:121]\n" \
  "v_pk_fma_f32 v[198:199], v[122:123], v[122:123], v[198:199]\n" \
  "v_pk_mul_f32 v[192:193], v[124:125], v[124:125]\n" \
  "v_pk_mul_f32 v[194:195], v[126:127], v[126:127]\n" \
  "v_pk_fma_f32 v[192:193], v[192:193], v[172:173], v[170:171] op_sel_hi:[1,0,0]\n" \
  "v_pk_fma_f32 v[194:195], v[194:195], v[172:173], v[170:171] op_sel_hi:[1,0,0]\n" \
  "v_pk_mul_f32 v[192:193], v[192:193], v[124:125]\n" \
  "v_pk_mul_f32 v[194:195], v[194:195], v[126:127]\n" \
  "s_waitcnt lgkmcnt(2)\n" \
  "v_mfma_f32_16x16x32_bf16 v[128:131], a[160:163], v[80:83], 0\n" \
  "buffer_load_dwordx4 a[52:55], v227, s[44:47], s58 offen offset:1024 sc1\n" \
  "v_exp_f32 v192, v192\n" \
  "v_mfma_f32_16x16x32_bf16 v[128:131], a[164:167], v[84:87], v[128:131]\n" \
  "ds_read_b128 v[28:31], v224 offset:5024\n" \
  "v_exp_f32 v193, v193\n" \
  "v_mfma_f32_16x16x32_bf16 v[128:131], a[168:171], v[88:91], v[128:131]\n" \
  "v_exp_f32 v194, v194\n" \
  "v_mfma_f32_16x16x32_bf16 v[128:131], a[172:175], v[92:95], v[128:131]\n" \
  "ds_read_b128 v[32:35], v224 offset:5088\n" \
  "v_exp_f32 v195, v195\n" \
  "v_pk_add_f32 v[192:193], v[192:193], 1.0 op_sel_hi:[1,0]\n" \
  "v_pk_add_f32 v[194:195], v[194:195], 1.0 op_sel_hi:[1,0]\n" \
  "v_mfma_f32_16x16x32_bf16 v[128:131], a[176:179], v[96:99], v[128:131]\n" \
  "buffer_load_dwordx4 a[56:59], v227, s[44:47], s58 offen offset:2048 sc1\n" \
  "v_rcp_f32 v192, v192\n" \
  "v_rcp_f32 v193, v193\n" \
  "v_rcp_f32 v194, v194\n" \
  "v_rcp_f32 v195, v195\n" \
  "v_pk_mul_f32 v[124:125], v[124:125], v[192:193]\n" \
  "v_pk_mul_f32 v[126:127], v[126:127], v[194:195]\n" \
  "v_pk_add_f32 v[200:201], v[124:125], v[126:127]\n" \
  "v_pk_mul_f32 v[202:203], v[124:125], v[124:125]\n" \
  "v_pk_fma_f32 v[202:203], v[126:127], v[126:127], v[202:203]\n" \
  "s_cmp_ge_u32 %[w], 2\n" \
  "s_cbranch_scc1 W81%=\n" \
  "buffer_load_dwordx4 a[72:75], v227, s[44:47], s59 offen offset:0 sc1\n" \
  "buffer_load_dwordx4 a[76:79], v227, s[44:47], s59 offen offset:1024 sc1\n" \
  "buffer_load_dwordx4 a[80:83], v227, s[44:47], s59 offen offset:2048 sc1\n" \
  "W81%=:\n" \
  "v_pk_mul_f32 v[192:193], v[128:129], v[128:129]\n" \
  "v_pk_mul_f32 v[194:195], v[130:131], v[130:131]\n" \
  "v_pk_fma_f32 v[192:193], v[192:193], v[172:173], v[170:171] op_sel_hi:[1,0,0]\n" \
  "v_pk_fma_f32 v[194:195], v[194:195], v[172:173], v[170:171] op_sel_hi:[1,0,0]\n" \
  "v_pk_mul_f32 v[192:193], v[192:193], v[128:129]\n" \
  "v_pk_mul_f32 v[194:195], v[194:195], v[130:131]\n" \
  "v_mfma_f32_16x16x32_bf16 v[144:147], a[144:147], v[0:3], 0\n" \
  "buffer_load_dwordx4 a[60:63], v227, s[44:47], s58 offen offset:3072 sc1\n" \
  "v_exp_f32 v192, v192\n" \
  "v_mfma_f32_16x16x32_bf16 v[144:147], a[148:151], v[4:7], v[144:147]\n" \
  "ds_read_b128 v[36:39], v168\n" \
  "v_exp_f32 v193, v193\n" \
  "v_mfma_f32_16x16x32_bf16 v[144:147], a[152:155], v[8:11], v[144:147]\n" \
  "v_exp_f32 v194, v194\n" \
  "v_mfma_f32_16x16x32_bf16 v[144:147], a[156:159], v[12:15], v[144:147]\n" \
  "v_exp_f32 v195, v195\n" \
  "v_pk_add_f32 v[192:193], v[192:193], 1.0 op_sel_hi:[1,0]\n" \
  "v_pk_add_f32 v[194:195], v[194:195], 1.0 op_sel_hi:[1,0]\n" \
  "v_mfma_f32_16x16x32_bf16 v[144:147], a[176:179], v[16:19], v[144:147]\n" \
  "s_add_u32 s58, s58, 0x1000\n" \
  "buffer_load_dwordx4 a[64:67], v227, s[44:47], s58 offen offset:0 sc1\n" \
  "v_rcp_f32 v192, v192\n" \
  "v_rcp_f32 v193, v193\n" \
  "v_rcp_f32 v194, v194\n" \
  "v_rcp_f32 v195, v195\n" \
  "v_pk_mul_f32 v[128:129], v[128:129], v[192:193]\n" \
  "v_pk_mul_f32 v[130:131], v[130:131], v[194:195]\n" \
  "v_pk_add_f32 v[204:205], v[128:129], v[130:131]\n" \
  "v_pk_mul_f32 v[206:207], v[128:129], v[128:129]\n" \
  "v_pk_fma_f32 v[206:207], v[130:131], v[130:131], v[206:207]\n" \
  "v_pk_mul_f32 v[192:193], v[144:145], v[144:145]\n" \
  "v_pk_mul_f32 v[194:195], v[146:147], v[146:147]\n" \
  "v_pk_fma_f32 v[192:193], v[192:193], v[172:173], v[170:171] op_sel_hi:[1,0,0]\n" \
  "v_pk_fma_f32 v[194:195], v[194:195], v[172:173], v[170:171] op_sel_hi:[1,0,0]\n" \
  "v_pk_mul_f32 v[192:193], v[192:193], v[144:145]\n" \
  "v_pk_mul_f32 v[194:195], v[194:195], v[146:147]\n" \
  "s_waitcnt lgkmcnt(1)\n" \
  "v_mfma_f32_16x16x32_bf16 v[148:151], a[144:147], v[20:23], 0\n" \
  "buffer_load_dwordx4 a[68:71], v227, s[44:47], s58 offen offset:1024 sc1\n" \
  "v_exp_f32 v192, v192\n" \
  "v_mfma_f32_16x16x32_bf16 v[148:151], a[148:151], v[24:27], v[148:151]\n" \
  "v_exp_f32 v193, v193\n" \
  "v_mfma_f32_16x16x32_bf16 v[148:151], a[152:155], v[28:31], v[148:151]\n" \
  "v_exp_f32 v194, v194\n" \
  "v_mfma_f32_16x16x32_bf16 v[148:151], a[156:159], v[32:35], v[148:151]\n" \
  "v_exp_f32 v195, v195\n" \
  "v_pk_add_f32 v[192:193], v[192:193], 1.0 op_sel_hi:[1,0]\n" \
  "v_pk_add_f32 v[194:195], v[194:195], 1.0 op_sel_hi:[1,0]\n" \
  "s_waitcnt lgkmcnt(0)\n" \
  "v_mfma_f32_16x16x32_bf16 v[148:151], a[176:179], v[36:39], v[148:151]\n" \
  "global_load_dwordx4 v[212:215], v244, s[74:75]\n" \
  "v_rcp_f32 v192, v192\n" \
  "v_rcp_f32 v193, v193\n" \
  "v_rcp_f32 v194, v194\n" \
  "v_rcp_f32 v195, v195\n" \
  "v_pk_mul_f32 v[144:145], v[144:145], v[192:193]\n" \
  "v_pk_mul_f32 v[146:147], v[146:147], v[194:195]\n" \
  "s_cmp_ge_u32 %[w], 2\n" \
  "s_cbranch_scc1 W83%=\n" \
  "buffer_load_dwordx4 a[84:87], v227, s[44:47], s59 offen offset:3072 sc1\n" \
  "s_add_u32 s59, s59, 0x1000\n" \
  "buffer_load_dwordx4 a[88:91], v227, s[44:47], s59 offen offset:0 sc1\n" \
  "buffer_load_dwordx4 a[92:95], v227, s[44:47], s59 offen offset:1024 sc1\n" \
  "W83%=:\n" \
  "v_pk_mul_f32 v[192:193], v[148:149], v[148:149]\n" \
  "v_pk_mul_f32 v[194:195], v[150:151], v[150:151]\n" \
  "v_pk_fma_f32 v[192:193], v[192:193], v[172:173], v[170:171] op_sel_hi:[1,0,0]\n" \
  "v_pk_fma_f32 v[194:195], v[194:195], v[172:173], v[170:171] op_sel_hi:[1,0,0]\n" \
  "v_pk_mul_f32 v[192:193], v[192:193], v[148:149]\n" \
  "v_pk_mul_f32 v[194:195], v[194:195], v[150:151]\n" \
  "v_mfma_f32_16x16x32_bf16 v[132:135], a[196:199], v[40:43], 0\n" \
  "global_load_dwordx4 v[216:219], v245, s[74:75]\n" \
  "v_exp_f32 v192, v192\n" \
  "v_mfma_f32_16x16x32_bf16 v[132:135], a[200:203], v[44:47], v[132:135]\n" \
  "v_exp_f32 v193, v193\n" \
  "v_mfma_f32_16x16x32_bf16 v[132:135], a[204:207], v[48:51], v[132:135]\n" \
  "v_exp_f32 v194, v194\n" \
  "v_mfma_f32_16x16x32_bf16 v[132:135], a[208:211], v[52:55], v[132:135]\n" \
  "v_exp_f32 v195, v195\n" \
  "v_pk_add_f32 v[192:193], v[192:193], 1.0 op_sel_hi:[1,0]\n" \
  "v_pk_add_f32 v[194:195], v[194:195], 1.0 op_sel_hi:[1,0]\n" \
  "v_mfma_f32_16x16x32_bf16 v[132:135], a[212:215], v[56:59], v[132:135]\n" \
  "global_load_dwordx2 v[220:221], v246, s[74:75]\n" \
  "v_rcp_f32 v192, v192\n" \
  "v_rcp_f32 v193, v193\n" \
  "v_rcp_f32 v194, v194\n" \
  "v_rcp_f32 v195, v195\n" \
  "v_pk_mul_f32 v[148:149], v[148:149], v[192:193]\n" \
  "v_pk_mul_f32 v[150:151], v[150:151], v[194:195]\n" \
  "s_nop 0\n" \
  "v_pk_mul_f32 v[192:193], v[132:133], v[132:133]\n" \
  "v_pk_mul_f32 v[194:195], v[134:135], v[134:135]\n" \
  "v_pk_fma_f32 v[192:193], v[192:193], v[172:173], v[170:171] op_sel_hi:[1,0,0]\n" \
  "v_pk_fma_f32 v[194:195], v[194:195], v[172:173], v[170:171] op_sel_hi:[1,0,0]\n" \
  "v_pk_mul_f32 v[192:193], v[192:193], v[132:133]\n" \
  "v_pk_mul_f32 v[194:195], v[194:195], v[134:135]\n" \
  "v_mfma_f32_16x16x32_bf16 v[136:139], a[196:199], v[60:63], 0\n" \
  "v_exp_f32 v192, v192\n" \
  "v_mfma_f32_16x16x32_bf16 v[136:139], a[200:203], v[64:67], v[136:139]\n" \
  "v_exp_f32 v193, v193\n" \
  "v_mfma_f32_16x16x32_bf16 v[136:139], a[204:207], v[68:71], v[136:139]\n" \
  "v_exp_f32 v194, v194\n" \
  "v_mfma_f32_16x16x32_bf16 v[136:139], a[208:211], v[72:75], v[136:139]\n" \
  "v_exp_f32 v195, v195\n" \
  "v_pk_add_f32 v[192:193], v[192:193], 1.0 op_sel_hi:[1,0]\n" \
  "v_pk_add_f32 v[194:195], v[194:195], 1.0 op_sel_hi:[1,0]\n" \
  "v_mfma_f32_16x16x32_bf16 v[136:139], a[212:215], v[76:79], v[136:139]\n" \
  "v_rcp_f32 v192, v192\n" \
  "v_rcp_f32 v193, v193\n" \
  "v_rcp_f32 v194, v194\n" \
  "v_rcp_f32 v195, v195\n" \
  "v_pk_mul_f32 v[132:133], v[132:133], v[192:193]\n" \
  "v_pk_mul_f32 v[134:135], v[134:135], v[194:195]\n" \
  "v_pk_add_f32 v[196:197], v[196:197], v[132:133]\n" \
  "v_pk_fma_f32 v[198:199], v[132:133], v[132:133], v[198:199]\n" \
  "v_pk_add_f32 v[196:197], v[196:197], v[134:135]\n" \
  "v_pk_fma_f32 v[198:199], v[134:135], v[134:135], v[198:199]\n" \
  "s_cmp_ge_u32 %[w], 2\n" \
  "s_cbranch_scc1 W85%=\n" \
  "buffer_load_dwordx4 a[96:99], v227, s[44:47], s59 offen offset:2048 sc1\n" \
  "buffer_load_dwordx4 a[100:103], v227, s[44:47], s59 offen offset:3072 sc1\n" \
  "s_add_u32 s59, s59, 0x1000\n" \
  "buffer_load_dwordx4 a[104:107], v227, s[44:47], s59 offen offset:0 sc1\n" \
  "W85%=:\n" \
  "v_pk_mul_f32 v[192:193], v[136:137], v[136:137]\n" \
  "v_pk_mul_f32 v[194:195], v[138:139], v[138:139]\n" \
  "v_pk_fma_f32 v[192:193], v[192:193], v[172:173], v[170:171] op_sel_hi:[1,0,0]\n" \
  "v_pk_fma_f32 v[194:195], v[194:195], v[172:173], v[170:171] op_sel_hi:[1,0,0]\n" \
  "v_pk_mul_f32 v[192:193], v[192:193], v[136:137]\n" \
  "v_pk_mul_f32 v[194:195], v[194:195], v[138:139]\n" \
  "v_mfma_f32_16x16x32_bf16 v[140:143], a[196:199], v[80:83], 0\n" \
  "v_exp_f32 v192, v192\n" \
  "v_mfma_f32_16x16x32_bf16 v[140:143], a[200:203], v[84:87], v[140:143]\n" \
  "v_exp_f32 v193, v193\n" \
  "v_mfma_f32_16x16x32_bf16 v[140:143], a[204:207], v[88:91], v[140:143]\n" \
  "v_exp_f32 v194, v194\n" \
  "v_mfma_f32_16x16x32_bf16 v[140:143], a[208:211], v[92:95], v[140:143]\n" \
  "v_exp_f32 v195, v195\n" \
  "v_pk_add_f32 v[192:193], v[192:193], 1.0 op_sel_hi:[1,0]\n" \
  "v_pk_add_f32 v[194:195], v[194:195], 1.0 op_sel_hi:[1,0]\n" \
  "v_mfma_f32_16x16x32_bf16 v[140:143], a[212:215], v[96:99], v[140:143]\n" \
  "v_rcp_f32 v192, v192\n" \
  "v_rcp_f32 v193, v193\n" \
  "v_rcp_f32 v194, v194\n" \
  "v_rcp_f32 v195, v195\n" \
  "v_pk_mul_f32 v[136:137], v[136:137], v[192:193]\n" \
  "v_pk_mul_f32 v[138:139], v[138:139], v[194:195]\n" \
  "v_pk_add_f32 v[200:201], v[200:201], v[136:137]\n" \
  "v_pk_fma_f32 v[202:203], v[136:137], v[136:137], v[202:203]\n" \
  "v_pk_add_f32 v[200:201], v[200:201], v[138:139]\n" \
  "v_pk_fma_f32 v[202:203], v[138:139], v[138:139], v[202:203]\n" \
  "v_pk_mul_f32 v[192:193], v[140:141], v[140:141]\n" \
  "v_pk_mul_f32 v[194:195], v[142:143], v[142:143]\n" \
  "v_pk_fma_f32 v[192:193], v[192:193], v[172:173], v[170:171] op_sel_hi:[1,0,0]\n" \
  "v_pk_fma_f32 v[194:195], v[194:195], v[172:173], v[170:171] op_sel_hi:[1,0,0]\n" \
  "v_pk_mul_f32 v[192:193], v[192:193], v[140:141]\n" \
  "v_pk_mul_f32 v[194:195], v[194:195], v[142:143]\n" \
  "v_mfma_f32_16x16x32_bf16 v[152:155], a[180:183], v[0:3], 0\n" \
  "v_exp_f32 v192, v192\n" \
  "v_mfma_f32_16x16x32_bf16 v[152:155], a[184:187], v[4:7], v[152:155]\n" \
  "v_exp_f32 v193, v193\n" \
  "v_mfma_f32_16x16x32_bf16 v[152:155], a[188:191], v[8:11], v[152:155]\n" \
  "v_exp_f32 v194, v194\n" \
  "v_mfma_f32_16x16x32_bf16 v[152:155], a[192:195], v[12:15], v[152:155]\n" \
  "v_exp_f32 v195, v195\n" \
  "v_pk_add_f32 v[192:193], v[192:193], 1.0 op_sel_hi:[1,0]\n" \
  "v_pk_add_f32 v[194:195], v[194:195], 1.0 op_sel_hi:[1,0]\n" \
  "v_mfma_f32_16x16x32_bf16 v[152:155], a[212:215], v[16:19], v[152:155]\n" \
  "v_rcp_f32 v192, v192\n" \
  "v_rcp_f32 v193, v193\n" \
  "v_rcp_f32 v194, v194\n" \
  "v_rcp_f32 v195, v195\n" \
  "v_pk_mul_f32 v[140:141], v[140:141], v[192:193]\n" \
  "v_pk_mul_f32 v[142:143], v[142:143], v[194:195]\n" \
  "v_pk_add_f32 v[204:205], v[204:205], v[140:141]\n" \
  "v_pk_fma_f32 v[206:207], v[140:141], v[140:141], v[206:207]\n" \
  "v_pk_add_f32 v[204:205], v[204:205], v[142:143]\n" \
  "v_pk_fma_f32 v[206:207], v[142:143], v[142:143], v[206:207]\n" \
  "v_pk_mul_f32 v[192:193], v[152:153], v[152:153]\n" \
  "v_pk_mul_f32 v[194:195], v[154:155], v[154:155]\n" \
  "v_pk_fma_f32 v[192:193], v[192:193], v[172:173], v[170:171] op_sel_hi:[1,0,0]\n" \
  "v_pk_fma_f32 v[194:195], v[194:195], v[172:173], v[170:171] op_sel_hi:[1,0,0]\n" \
  "v_pk_mul_f32 v[192:193], v[192:193], v[152:153]\n" \
  "v_pk_mul_f32 v[194:195], v[194:195], v[154:155]\n" \
  "v_mfma_f32_16x16x32_bf16 v[156:159], a[180:183], v[20:23], 0\n" \
  "v_exp_f32 v192, v192\n" \
  "v_mfma_f32_16x16x32_bf16 v[156:159], a[184:187], v[24:27], v[156:159]\n" \
  "v_exp_f32 v193, v193\n" \
  "v_mfma_f32_16x16x32_bf16 v[156:159], a[188:191], v[28:31], v[156:159]\n" \
  "v_exp_f32 v194, v194\n" \
  "v_mfma_f32_16x16x32_bf16 v[156:159], a[192:195], v[32:35], v[156:159]\n" \
  "v_exp_f32 v195, v195\n" \
  "v_pk_add_f32 v[192:193], v[192:193], 1.0 op_sel_hi:[1,0]\n" \
  "v_pk_add_f32 v[194:195], v[194:195], 1.0 op_sel_hi:[1,0]\n" \
  "v_mfma_f32_16x16x32_bf16 v[156:159], a[212:215], v[36:39], v[156:159]\n" \
  "v_rcp_f32 v192, v192\n" \
  "v_rcp_f32 v193, v193\n" \
  "v_rcp_f32 v194, v194\n" \
  "v_rcp_f32 v195, v195\n" \
  "v_pk_mul_f32 v[152:153], v[152:153], v[192:193]\n" \
  "v_pk_mul_f32 v[154:155], v[154:155], v[194:195]\n" \
  "s_nop 1\n" \
  "v_pk_mul_f32 v[192:193], v[156:157], v[156:157]\n" \
  "v_pk_mul_f32 v[194:195], v[158:159], v[158:159]\n" \
  "v_pk_fma_f32 v[192:193], v[192:193], v[172:173], v[170:171] op_sel_hi:[1,0,0]\n" \
  "v_pk_fma_f32 v[194:195], v[194:195], v[172:173], v[170:171] op_sel_hi:[1,0,0]\n" \
  "v_pk_mul_f32 v[192:193], v[192:193], v[156:157]\n" \
  "v_pk_mul_f32 v[194:195], v[194:195], v[158:159]\n" \
  "v_exp_f32 v192, v192\n" \
  "v_exp_f32 v193, v193\n" \
  "v_exp_f32 v194, v194\n" \
  "v_exp_f32 v195, v195\n" \
  "v_pk_add_f32 v[192:193], v[192:193], 1.0 op_sel_hi:[1,0]\n" \
  "v_pk_add_f32 v[194:195], v[194:195], 1.0 op_sel_hi:[1,0]\n" \
  "v_rcp_f32 v192, v192\n" \
  "v_rcp_f32 v193, v193\n" \
  "v_rcp_f32 v194, v194\n" \
  "v_rcp_f32 v195, v195\n" \
  "v_pk_mul_f32 v[156:157], v[156:157], v[192:193]\n" \
  "v_pk_mul_f32 v[158:159], v[158:159], v[194:195]\n" \
  "s_lshl_b32 s64, %[w], 7\n" \
  "s_sub_u32 s64, 512, s64\n" \
  "v_add_u32 v175, s64, v247\n" \
  "ds_read_b128 v[48:51], v247 offset:0\n" \
  "ds_read_b128 v[52:55], v247 offset:576\n" \
  "ds_read_b128 v[56:59], v247 offset:1152\n" \
  "ds_read_b128 v[60:63], v247 offset:64\n" \
  "ds_read_b128 v[64:67], v247 offset:640\n" \
  "ds_read_b128 v[68:71], v247 offset:1216\n" \
  "ds_read_b128 v[72:75], v175 offset:0\n" \
  "ds_read_b128 v[76:79], v175 offset:576\n" \
  "ds_read_b128 v[80:83], v175 offset:1152\n" \
  "v_add_f32 v196, v196, v197\n" \
  "v_add_f32 v198, v198, v199\n" \
  "v_add_f32 v200, v200, v201\n" \
  "v_add_f32 v202, v202, v203\n" \
  "v_add_f32 v204, v204, v205\n" \
  "v_add_f32 v206, v206, v207\n" \
  "v_add_u32 v174, s62, v174\n" \
  "s_sub_u32 s64, 3, %[w]\n" \
  "v_permlane32_swap_b32 v196, v204\n" \
  "v_permlane32_swap_b32 v200, v208\n" \
  "v_permlane32_swap_b32 v198, v206\n" \
  "v_permlane32_swap_b32 v202, v210\n" \
  "v_add_f32 v196, v196, v204\n" \
  "v_add_f32 v200, v200, v208\n" \
  "v_add_f32 v198, v198, v206\n" \
  "v_add_f32 v202, v202, v210\n" \
  "v_add_u32 v226, 157312, v226\n" \
  "s_and_b32 s64, s64, 1\n" \
  "v_permlane16_swap_b32 v196, v200\n" \
  "v_permlane16_swap_b32 v198, v202\n" \
  "s_lshl_b32 s65, s64, 10\n" \
  "s_add_u32 s65, s65, 101376\n" \
  "v_add_f32 v196, v196, v200\n" \
  "v_add_f32 v197, v198, v202\n" \
  "ds_write_b64 v174, v[196:197]\n" \
  "v_add_u32 v225, s65, v227\n" \
  "s_lshl_b32 s65, s64, 11\n" \
  "s_sub_u32 s65, 4096, s65\n" \
  "v_add_u32 v175, s65, v225\n" \
  "s_cmp_eq_u32 s64, 0\n" \
  "s_cselect_b64 s[68:69], -1, 0\n" \
  "s_waitcnt lgkmcnt(0)\n" \
  "s_barrier\n" \
  "ds_read_b128 v[0:3], v226 offset:0\n" \
  "ds_read_b128 v[4:7], v226 offset:16\n" \
  "ds_read_b64 v[8:9], v226 offset:32\n" \
  "ds_read_b128 v[12:15], v226 offset:768\n" \
  "ds_read_b128 v[16:19], v226 offset:784\n" \
  "ds_read_b64 v[20:21], v226 offset:800\n" \
  "ds_read_b128 v[24:27], v226 offset:1536\n" \
  "ds_read_b128 v[28:31], v226 offset:1552\n" \
  "ds_read_b64 v[32:33], v226 offset:1568\n" \
  "ds_read_b128 a[144:147], v228 offset:0\n" \
  "ds_read_b128 a[148:151], v228 offset:1024\n" \
  "ds_read_b64 a[140:141], v229 offset:4096\n" \
  "ds_read_b128 a[152:155], v228 offset:9216\n" \
  "ds_read_b128 a[156:159], v228 offset:10240\n" \
  "ds_read_b64 a[142:143], v229 offset:13312\n" \
  "buffer_load_dwordx4 v[176:179], v227, s[44:47], s72 offen sc1\n" \
  "buffer_load_dwordx4 v[180:183], v227, s[44:47], s72 offen offset:1024 sc1\n" \
  "buffer_load_dwordx4 v[184:187], v227, s[44:47], s73 offen sc1\n" \
  "v_mov_b32 v84, 0.006944444444444444\n" \
  "v_mov_b32 v222, 0\n" \
  "v_mov_b32 v223, 0\n" \
  "s_waitcnt vmcnt(5)\n" \
  "v_mfma_f32_16x16x32_bf16 v[100:103], v[238:241], v[212:215], 0\n" \
  "s_waitcnt vmcnt(4)\n" \
  "v_mfma_f32_16x16x32_bf16 v[104:107], v[238:241], v[216:219], 0\n" \
  "v_mfma_f32_16x16x32_bf16 v[108:111], v[240:243], v[212:215], 0\n" \
  "v_mfma_f32_16x16x32_bf16 v[112:115], v[240:243], v[216:219], 0\n" \
  "s_waitcnt vmcnt(3)\n" \
  "v_mfma_f32_16x16x32_bf16 v[116:119], v[238:241], v[220:223], 0\n" \
  "ds_read_b128 a[160:163], v228 offset:18432\n" \
  "ds_read_b128 a[164:167], v228 offset:19456\n" \
  "ds_read_b64 a[252:253], v229 offset:22528\n" \
  "ds_read_b128 a[168:171], v228 offset:27648\n" \
  "ds_read_b128 a[172:175], v228 offset:28672\n" \
  "ds_read_b64 a[254:255], v229 offset:31744\n" \
  "ds_read_b128 v[36:39], v225\n" \
  "ds_read_b128 v[40:43], v225 offset:1024\n" \
  "ds_read_b128 v[44:47], v175\n" \
  "s_waitcnt lgkmcnt(15)\n" \
  "v_pk_add_f32 v[0:1], v[0:1], v[2:3]\n" \
  "v_pk_add_f32 v[12:13], v[12:13], v[14:15]\n" \
  "v_pk_add_f32 v[24:25], v[24:25], v[26:27]\n" \
  "v_pk_add_f32 v[4:5], v[4:5], v[6:7]\n" \
  "v_pk_add_f32 v[16:17], v[16:17], v[18:19]\n" \
  "v_pk_add_f32 v[28:29], v[28:29], v[30:31]\n" \
  "v_pk_add_f32 v[0:1], v[0:1], v[8:9]\n" \
  "v_pk_add_f32 v[12:13], v[12:13], v[20:21]\n" \
  "v_pk_add_f32 v[24:25], v[24:25], v[32:33]\n" \
  "v_pk_add_f32 v[0:1], v[0:1], v[4:5]\n" \
  "v_pk_add_f32 v[12:13], v[12:13], v[16:17]\n" \
  "v_pk_add_f32 v[24:25], v[24:25], v[28:29]\n" \
  "v_pk_mul_f32 v[0:1], v[0:1], v[84:85] op_sel_hi:[1,0]\n" \
  "v_pk_mul_f32 v[12:13], v[12:13], v[84:85] op_sel_hi:[1,0]\n" \
  "v_pk_mul_f32 v[24:25], v[24:25], v[84:85] op_sel_hi:[1,0]\n" \
  "s_waitcnt lgkmcnt(9)\n" \
  "v_mfma_f32_16x16x32_bf16 v[192:195], v[238:241], a[144:147], 0\n" \
  "v_fma_f32 v1, -v0, v0, v1\n" \
  "v_fma_f32 v13, -v12, v12, v13\n" \
  "v_fma_f32 v25, -v24, v24, v25\n" \
  "v_mfma_f32_16x16x32_bf16 v[196:199], v[240:243], a[144:147], 0\n" \
  "v_max_f32 v1, 0, v1\n" \
  "v_max_f32 v13, 0, v13\n" \
  "v_max_f32 v25, 0, v25\n" \
  "v_mfma_f32_16x16x32_bf16 v[200:203], v[238:241], a[148:151], 0\n" \
  "v_add_f32 v1, 1e-05, v1\n" \
  "v_add_f32 v13, 1e-05, v13\n" \
  "v_add_f32 v25, 1e-05, v25\n" \
  "v_mfma_f32_16x16x32_bf16 v[204:207], v[240:243], a[148:151], 0\n" \
  "v_rsq_f32 v2, v1\n" \
  "v_rsq_f32 v14, v13\n" \
  "v_rsq_f32 v26, v25\n" \
  "v_mfma_f32_16x16x32_bf16 v[192:195], v[238:241], a[152:155], v[192:195]\n" \
  "v_cndmask_b32 v2, 0, v2, s[76:77]\n" \
  "v_cndmask_b32 v26, 0, v26, s[78:79]\n" \
  "v_mfma_f32_16x16x32_bf16 v[196:199], v[240:243], a[152:155], v[196:199]\n" \
  "v_mul_f32 v4, -v0, v2\n" \
  "v_mul_f32 v16, -v12, v14\n" \
  "v_mul_f32 v28, -v24, v26\n" \
  "v_pk_fma_f32 v[120:121], v[120:121], v[2:3], v[4:5] op_sel_hi:[1,0,0]\n" \
  "v_pk_fma_f32 v[122:123], v[122:123], v[2:3], v[4:5] op_sel_hi:[1,0,0]\n" \
  "v_pk_fma_f32 v[124:125], v[124:125], v[14:15], v[16:17] op_sel_hi:[1,0,0]\n" \
  "v_pk_fma_f32 v[126:127], v[126:127], v[14:15], v[16:17] op_sel_hi:[1,0,0]\n" \
  "v_pk_fma_f32 v[128:129], v[128:129], v[26:27], v[28:29] op_sel_hi:[1,0,0]\n" \
  "v_pk_fma_f32 v[130:131], v[130:131], v[26:27], v[28:29] op_sel_hi:[1,0,0]\n" \
  "v_pk_mul_f32 v[88:89], v[120:121], v[48:49]\n" \
  "v_pk_mul_f32 v[90:91], v[122:123], v[50:51]\n" \
  "v_pk_mul_f32 v[92:93], v[124:125], v[48:49]\n" \
  "v_pk_mul_f32 v[94:95], v[126:127], v[50:51]\n" \
  "v_mfma_f32_16x16x32_bf16 v[200:203], v[238:241], a[156:159], v[200:203]\n" \
  "v_fmac_f32_dpp v88, v120, v52 row_shl:1 row_mask:0xf bank_mask:0xf bound_ctrl:1\n" \
  "v_fmac_f32_dpp v89, v121, v53 row_shl:1 row_mask:0xf bank_mask:0xf bound_ctrl:1\n" \
  "v_mfma_f32_16x16x32_bf16 v[204:207], v[240:243], a[156:159], v[204:207]\n" \
  "v_fmac_f32_dpp v90, v122, v54 row_shl:1 row_mask:0xf bank_mask:0xf bound_ctrl:1\n" \
  "v_fmac_f32_dpp v91, v123, v55 row_shl:1 row_mask:0xf bank_mask:0xf bound_ctrl:1\n" \
  "v_mfma_f32_16x16x32_bf16 v[208:211], v[236:239], a[140:143], 0\n" \
  "v_fmac_f32_dpp v92, v124, v52 row_shl:1 row_mask:0xf bank_mask:0xf bound_ctrl:1\n" \
  "v_fmac_f32_dpp v93, v125, v53 row_shl:1 row_mask:0xf bank_mask:0xf bound_ctrl:1\n" \
  "s_waitcnt lgkmcnt(0)\n" \
  "v_mfma_f32_16x16x32_bf16 v[192:195], v[238:241], a[160:163], v[192:195]\n" \
  "v_fmac_f32_dpp v94, v126, v54 row_shl:1 row_mask:0xf bank_mask:0xf bound_ctrl:1\n" \
  "v_fmac_f32_dpp v95, v127, v55 row_shl:1 row_mask:0xf bank_mask:0xf bound_ctrl:1\n" \
  "v_mfma_f32_16x16x32_bf16 v[196:199], v[240:243], a[160:163], v[196:199]\n" \
  "v_fmac_f32_dpp v88, v124, v52 row_shr:15 row_mask:0xf bank_mask:0xf bound_ctrl:1\n" \
  "v_fmac_f32_dpp v89, v125, v53 row_shr:15 row_mask:0xf bank_mask:0xf bound_ctrl:1\n" \
  "v_mfma_f32_16x16x32_bf16 v[200:203], v[238:241], a[164:167], v[200:203]\n" \
  "v_fmac_f32_dpp v90, v126, v54 row_shr:15 row_mask:0xf bank_mask:0xf bound_ctrl:1\n" \
  "v_fmac_f32_dpp v91, v127, v55 row_shr:15 row_mask:0xf bank_mask:0xf bound_ctrl:1\n" \
  "v_mfma_f32_16x16x32_bf16 v[204:207], v[240:243], a[164:167], v[204:207]\n" \
  "v_fmac_f32_dpp v92, v128, v52 row_shr:15 row_mask:0xf bank_mask:0xf bound_ctrl:1\n" \
  "v_fmac_f32_dpp v93, v129, v53 row_shr:15 row_mask:0xf bank_mask:0xf bound_ctrl:1\n" \
  "v_mfma_f32_16x16x32_bf16 v[192:195], v[238:241], a[168:171], v[192:195]\n" \
  "v_fmac_f32_dpp v94, v130, v54 row_shr:15 row_mask:0xf bank_mask:0xf bound_ctrl:1\n" \
  "v_fmac_f32_dpp v95, v131, v55 row_shr:15 row_mask:0xf bank_mask:0xf bound_ctrl:1\n" \
  "v_mfma_f32_16x16x32_bf16 v[196:199], v[240:243], a[168:171], v[196:199]\n" \
  "v_fmac_f32_dpp v88, v120, v56 row_shl:2 row_mask:0xf bank_mask:0xf bound_ctrl:1\n" \
  "v_fmac_f32_dpp v89, v121, v57 row_shl:2 row_mask:0xf bank_mask:0xf bound_ctrl:1\n" \
  "v_mfma_f32_16x16x32_bf16 v[200:203], v[238:241], a[172:175], v[200:203]\n" \
  "v_fmac_f32_dpp v90, v122, v58 row_shl:2 row_mask:0xf bank_mask:0xf bound_ctrl:1\n" \
  "v_fmac_f32_dpp v91, v123, v59 row_shl:2 row_mask:0xf bank_mask:0xf bound_ctrl:1\n" \
  "v_mfma_f32_16x16x32_bf16 v[204:207], v[240:243], a[172:175], v[204:207]\n" \
  "v_fmac_f32_dpp v92, v124, v56 row_shl:2 row_mask:0xf bank_mask:0xf bound_ctrl:1\n" \
  "v_fmac_f32_dpp v93, v125, v57 row_shl:2 row_mask:0xf bank_mask:0xf bound_ctrl:1\n" \
  "v_mfma_f32_16x16x32_bf16 v[208:211], v[236:239], a[252:255], v[208:211]\n" \
  "v_fmac_f32_dpp v94, v126, v58 row_shl:2 row_mask:0xf bank_mask:0xf bound_ctrl:1\n" \
  "v_fmac_f32_dpp v95, v127, v59 row_shl:2 row_mask:0xf bank_mask:0xf bound_ctrl:1\n" \
  "v_fmac_f32_dpp v88, v124, v56 row_shr:14 row_mask:0xf bank_mask:0xf bound_ctrl:1\n" \
  "v_fmac_f32_dpp v89, v125, v57 row_shr:14 row_mask:0xf bank_mask:0xf bound_ctrl:1\n" \
  "v_fmac_f32_dpp v90, v126, v58 row_shr:14 row_mask:0xf bank_mask:0xf bound_ctrl:1\n" \
  "v_fmac_f32_dpp v91, v127, v59 row_shr:14 row_mask:0xf bank_mask:0xf bound_ctrl:1\n" \
  "v_fmac_f32_dpp v92, v128, v56 row_shr:14 row_mask:0xf bank_mask:0xf bound_ctrl:1\n" \
  "v_fmac_f32_dpp v93, v129, v57 row_shr:14 row_mask:0xf bank_mask:0xf bound_ctrl:1\n" \
  "v_fmac_f32_dpp v94, v130, v58 row_shr:14 row_mask:0xf bank_mask:0xf bound_ctrl:1\n" \
  "v_fmac_f32_dpp v95, v131, v59 row_shr:14 row_mask:0xf bank_mask:0xf bound_ctrl:1\n" \
  "v_pk_mul_f32 v[88:89], v[88:89], v[144:145]\n" \
  "v_pk_mul_f32 v[90:91], v[90:91], v[146:147]\n" \
  "v_pk_mul_f32 v[92:93], v[92:93], v[148:149]\n" \
  "v_pk_mul_f32 v[94:95], v[94:95], v[150:151]\n" \
  "v_cvt_pk_bf16_f32 v172, v88, v89\n" \
  "v_cvt_pk_bf16_f32 v173, v90, v91\n" \
  "v_cvt_pk_bf16_f32 v188, v92, v93\n" \
  "v_cvt_pk_bf16_f32 v189, v94, v95\n" \
  "s_mov_b32 s51, 262144\n" \
  "RC%=:\n" \
  "s_waitcnt vmcnt(0)\n" \
  "v_max3_u32 v144, v176, v177, v178\n" \
  "v_max3_u32 v145, v179, v180, v181\n" \
  "v_max3_u32 v146, v182, v183, v184\n" \
  "v_max3_u32 v144, v144, v185, v186\n" \
  "v_max3_u32 v145, v145, v146, v187\n" \
  "v_max_u32 v144, v144, v145\n" \
  "v_cmp_eq_u32 vcc, -1, v144\n" \
  "s_nop 0\n" \
  "s_cmp_eq_u64 vcc, 0\n" \
  "s_cbranch_scc1 RCD%=\n" \
  "buffer_load_dwordx4 v[176:179], v227, s[44:47], s72 offen sc1\n" \
  "buffer_load_dwordx4 v[180:183], v227, s[44:47], s72 offen offset:1024 sc1\n" \
  "buffer_load_dwordx4 v[184:187], v227, s[44:47], s73 offen sc1\n" \
  "s_sub_u32 s51, s51, 1\n" \
  "s_cmp_lg_u32 s51, 0\n" \
  "s_cbranch_scc1 RC%=\n" \
  "s_waitcnt vmcnt(0)\n" \
  "RCD%=:\n" \
  "v_pk_fma_f32 v[132:133], v[132:133], v[2:3], v[4:5] op_sel_hi:[1,0,0]\n" \
  "v_pk_fma_f32 v[134:135], v[134:135], v[2:3], v[4:5] op_sel_hi:[1,0,0]\n" \
  "v_pk_fma_f32 v[136:137], v[136:137], v[14:15], v[16:17] op_sel_hi:[1,0,0]\n" \
  "v_pk_fma_f32 v[138:139], v[138:139], v[14:15], v[16:17] op_sel_hi:[1,0,0]\n" \
  "v_pk_fma_f32 v[140:141], v[140:141], v[26:27], v[28:29] op_sel_hi:[1,0,0]\n" \
  "v_pk_fma_f32 v[142:143], v[142:143], v[26:27], v[28:29] op_sel_hi:[1,0,0]\n" \
  "v_pk_mul_f32 v[88:89], v[132:133], v[60:61]\n" \
  "v_pk_mul_f32 v[90:91], v[134:135], v[62:63]\n" \
  "v_pk_mul_f32 v[92:93], v[136:137], v[60:61]\n" \
  "v_pk_mul_f32 v[94:95], v[138:139], v[62:63]\n" \
  "v_mfma_f32_16x16x32_bf16 v[192:195], v[238:241], v[176:179], v[192:195]\n" \
  "v_fmac_f32_dpp v88, v132, v64 row_shl:1 row_mask:0xf bank_mask:0xf bound_ctrl:1\n" \
  "v_fmac_f32_dpp v89, v133, v65 row_shl:1 row_mask:0xf bank_mask:0xf bound_ctrl:1\n" \
  "v_mfma_f32_16x16x32_bf16 v[196:199], v[240:243], v[176:179], v[196:199]\n" \
  "v_fmac_f32_dpp v90, v134, v66 row_shl:1 row_mask:0xf bank_mask:0xf bound_ctrl:1\n" \
  "v_fmac_f32_dpp v91, v135, v67 row_shl:1 row_mask:0xf bank_mask:0xf bound_ctrl:1\n" \
  "v_mfma_f32_16x16x32_bf16 v[200:203], v[238:241], v[180:183], v[200:203]\n" \
  "v_fmac_f32_dpp v92, v136, v64 row_shl:1 row_mask:0xf bank_mask:0xf bound_ctrl:1\n" \
  "v_fmac_f32_dpp v93, v137, v65 row_shl:1 row_mask:0xf bank_mask:0xf bound_ctrl:1\n" \
  "v_mfma_f32_16x16x32_bf16 v[204:207], v[240:243], v[180:183], v[204:207]\n" \
  "v_fmac_f32_dpp v94, v138, v66 row_shl:1 row_mask:0xf bank_mask:0xf bound_ctrl:1\n" \
  "v_fmac_f32_dpp v95, v139, v67 row_shl:1 row_mask:0xf bank_mask:0xf bound_ctrl:1\n" \
  "v_mfma_f32_16x16x32_bf16 v[208:211], v[238:241], v[184:187], v[208:211]\n" \
  "v_fmac_f32_dpp v88, v136, v64 row_shr:15 row_mask:0xf bank_mask:0xf bound_ctrl:1\n" \
  "v_fmac_f32_dpp v89, v137, v65 row_shr:15 row_mask:0xf bank_mask:0xf bound_ctrl:1\n" \
  "v_fmac_f32_dpp v90, v138, v66 row_shr:15 row_mask:0xf bank_mask:0xf bound_ctrl:1\n" \
  "v_fmac_f32_dpp v91, v139, v67 row_shr:15 row_mask:0xf bank_mask:0xf bound_ctrl:1\n" \
  "v_fmac_f32_dpp v92, v140, v64 row_shr:15 row_mask:0xf bank_mask:0xf bound_ctrl:1\n" \
  "v_fmac_f32_dpp v93, v141, v65 row_shr:15 row_mask:0xf bank_mask:0xf bound_ctrl:1\n" \
  "v_fmac_f32_dpp v94, v142, v66 row_shr:15 row_mask:0xf bank_mask:0xf bound_ctrl:1\n" \
  "v_fmac_f32_dpp v95, v143, v67 row_shr:15 row_mask:0xf bank_mask:0xf bound_ctrl:1\n" \
  "s_mul_i32 s62, s66, 5120\n" \
  "s_lshl_b32 s63, s67, 11\n" \
  "s_add_u32 s62, s62, 83968\n" \
  "s_add_u32 s63, s63, s62\n" \
  "v_add_u32 v226, s63, v227\n" \
  "v_add_u32 v234, s62, v227\n" \
  "v_lshlrev_b32 v224, 2, v254\n" \
  "v_mov_b32 v120, 0\n" \
  "v_add_u32 v224, 160448, v224\n" \
  "v_mov_b32 v121, 0\n" \
  "s_lshl_b32 s62, s66, 6\n" \
  "v_cvt_pk_bf16_f32 v124, v192, v193\n" \
  "v_cvt_pk_bf16_f32 v125, v194, v195\n" \
  "v_cvt_pk_bf16_f32 v126, v196, v197\n" \
  "v_cvt_pk_bf16_f32 v127, v198, v199\n" \
  "v_cvt_pk_bf16_f32 v128, v200, v201\n" \
  "v_cvt_pk_bf16_f32 v129, v202, v203\n" \
  "v_cvt_pk_bf16_f32 v130, v204, v205\n" \
  "v_cvt_pk_bf16_f32 v131, v206, v207\n" \
  "v_cvt_pk_bf16_f32 v122, v208, v209\n" \
  "v_cvt_pk_bf16_f32 v123, v210, v211\n" \
  "v_add_u32 v223, s62, v224\n" \
  "s_cmp_eq_u32 s67, 0\n" \
  "ds_write_b128 v226, v[124:127]\n" \
  "ds_write_b128 v226, v[128:131] offset:1024\n" \
  "s_cselect_b64 exec, -1, 0\n" \
  "ds_write_b128 v234, v[120:123] offset:4096\n" \
  "s_cselect_b32 exec_hi, 0xffff0000, 0\n" \
  "s_mov_b32 exec_lo, 0\n" \
  "ds_write_b32 v223, v211\n" \
  "s_mov_b64 exec, -1\n" \
  "v_fmac_f32_dpp v88, v132, v68 row_shl:2 row_mask:0xf bank_mask:0xf bound_ctrl:1\n" \
  "v_fmac_f32_dpp v89, v133, v69 row_shl:2 row_mask:0xf bank_mask:0xf bound_ctrl:1\n" \
  "v_fmac_f32_dpp v90, v134, v70 row_shl:2 row_mask:0xf bank_mask:0xf bound_ctrl:1\n" \
  "v_fmac_f32_dpp v91, v135, v71 row_shl:2 row_mask:0xf bank_mask:0xf bound_ctrl:1\n" \
  "v_fmac_f32_dpp v92, v136, v68 row_shl:2 row_mask:0xf bank_mask:0xf bound_ctrl:1\n" \
  "v_fmac_f32_dpp v93, v137, v69 row_shl:2 row_mask:0xf bank_mask:0xf bound_ctrl:1\n" \
  "v_fmac_f32_dpp v94, v138, v70 row_shl:2 row_mask:0xf bank_mask:0xf bound_ctrl:1\n" \
  "v_fmac_f32_dpp v95, v139, v71 row_shl:2 row_mask:0xf bank_mask:0xf bound_ctrl:1\n" \
  "v_fmac_f32_dpp v88, v136, v68 row_shr:14 row_mask:0xf bank_mask:0xf bound_ctrl:1\n" \
  "v_fmac_f32_dpp v89, v137, v69 row_shr:14 row_mask:0xf bank_mask:0xf bound_ctrl:1\n" \
  "v_fmac_f32_dpp v90, v138, v70 row_shr:14 row_mask:0xf bank_mask:0xf bound_ctrl:1\n" \
  "v_fmac_f32_dpp v91, v139, v71 row_shr:14 row_mask:0xf bank_mask:0xf bound_ctrl:1\n" \
  "v_fmac_f32_dpp v92, v140, v68 row_shr:14 row_mask:0xf bank_mask:0xf bound_ctrl:1\n" \
  "v_fmac_f32_dpp v93, v141, v69 row_shr:14 row_mask:0xf bank_mask:0xf bound_ctrl:1\n" \
  "v_fmac_f32_dpp v94, v142, v70 row_shr:14 row_mask:0xf bank_mask:0xf bound_ctrl:1\n" \
  "v_fmac_f32_dpp v95, v143, v71 row_shr:14 row_mask:0xf bank_mask:0xf bound_ctrl:1\n" \
  "v_pk_mul_f32 v[88:89], v[88:89], v[152:153]\n" \
  "v_pk_mul_f32 v[90:91], v[90:91], v[154:155]\n" \
  "v_pk_mul_f32 v[92:93], v[92:93], v[156:157]\n" \
  "v_pk_mul_f32 v[94:95], v[94:95], v[158:159]\n" \
  "v_cvt_pk_bf16_f32 v174, v88, v89\n" \
  "v_cvt_pk_bf16_f32 v175, v90, v91\n" \
  "v_cvt_pk_bf16_f32 v190, v92, v93\n" \
  "v_cvt_pk_bf16_f32 v191, v94, v95\n" \
  "s_lshl_b32 s63, %[w], 10\n" \
  "s_add_u32 s63, s63, 73728\n" \
  "v_add_u32 v225, 73728, v227\n" \
  "v_add_u32 v226, s63, v227\n" \
  "ds_write_b128 v226, v[172:175] offset:0\n" \
  "ds_write_b128 v226, v[188:191] offset:5120\n" \
  "s_cmp_lt_u32 %[w], 2\n" \
  "s_cbranch_scc1 NC8%=\n" \
  "v_cndmask_b32 v2, v14, v2, s[68:69]\n" \
  "v_cndmask_b32 v4, v16, v4, s[68:69]\n" \
  "v_cndmask_b32 v14, v26, v14, s[68:69]\n" \
  "v_cndmask_b32 v16, v28, v16, s[68:69]\n" \
  "s_mul_i32 s64, s64, 5120\n" \
  "v_mov_b32 v154, 0\n" \
  "v_mov_b32 v155, 0\n" \
  "v_add_u32 v226, s64, v225\n" \
  "v_pk_fma_f32 v[36:37], v[36:37], v[2:3], v[4:5] op_sel_hi:[1,0,0]\n" \
  "v_pk_fma_f32 v[38:39], v[38:39], v[2:3], v[4:5] op_sel_hi:[1,0,0]\n" \
  "v_pk_fma_f32 v[40:41], v[40:41], v[14:15], v[16:17] op_sel_hi:[1,0,0]\n" \
  "v_pk_fma_f32 v[42:43], v[42:43], v[14:15], v[16:17] op_sel_hi:[1,0,0]\n" \
  "v_pk_mul_f32 v[88:89], v[36:37], v[72:73]\n" \
  "v_pk_mul_f32 v[90:91], v[38:39], v[74:75]\n" \
  "v_fmac_f32_dpp v88, v36, v76 row_shl:1 row_mask:0xf bank_mask:0xf bound_ctrl:1\n" \
  "v_fmac_f32_dpp v89, v37, v77 row_shl:1 row_mask:0xf bank_mask:0xf bound_ctrl:1\n" \
  "v_fmac_f32_dpp v90, v38, v78 row_shl:1 row_mask:0xf bank_mask:0xf bound_ctrl:1\n" \
  "v_fmac_f32_dpp v91, v39, v79 row_shl:1 row_mask:0xf bank_mask:0xf bound_ctrl:1\n" \
  "v_fmac_f32_dpp v88, v40, v76 row_shr:15 row_mask:0xf bank_mask:0xf bound_ctrl:1\n" \
  "v_fmac_f32_dpp v89, v41, v77 row_shr:15 row_mask:0xf bank_mask:0xf bound_ctrl:1\n" \
  "v_fmac_f32_dpp v90, v42, v78 row_shr:15 row_mask:0xf bank_mask:0xf bound_ctrl:1\n" \
  "v_fmac_f32_dpp v91, v43, v79 row_shr:15 row_mask:0xf bank_mask:0xf bound_ctrl:1\n" \
  "v_fmac_f32_dpp v88, v36, v80 row_shl:2 row_mask:0xf bank_mask:0xf bound_ctrl:1\n" \
  "v_fmac_f32_dpp v89, v37, v81 row_shl:2 row_mask:0xf bank_mask:0xf bound_ctrl:1\n" \
  "v_fmac_f32_dpp v90, v38, v82 row_shl:2 row_mask:0xf bank_mask:0xf bound_ctrl:1\n" \
  "v_fmac_f32_dpp v91, v39, v83 row_shl:2 row_mask:0xf bank_mask:0xf bound_ctrl:1\n" \
  "v_fmac_f32_dpp v88, v40, v80 row_shr:14 row_mask:0xf bank_mask:0xf bound_ctrl:1\n" \
  "v_fmac_f32_dpp v89, v41, v81 row_shr:14 row_mask:0xf bank_mask:0xf bound_ctrl:1\n" \
  "v_fmac_f32_dpp v90, v42, v82 row_shr:14 row_mask:0xf bank_mask:0xf bound_ctrl:1\n" \
  "v_fmac_f32_dpp v91, v43, v83 row_shr:14 row_mask:0xf bank_mask:0xf bound_ctrl:1\n" \
  "v_pk_mul_f32 v[88:89], v[88:89], v[44:45]\n" \
  "v_pk_mul_f32 v[90:91], v[90:91], v[46:47]\n" \
  "v_cvt_pk_bf16_f32 v152, v88, v89\n" \
  "v_cvt_pk_bf16_f32 v153, v90, v91\n" \
  "ds_write_b128 v226, v[152:155] offset:4096\n" \
  "NC8%=:\n" \
  "s_waitcnt lgkmcnt(0)\n" \
  "s_barrier\n" \
  "ds_read_b128 v[0:3], v225 offset:0\n" \
  "ds_read_b128 v[20:23], v225 offset:5120\n" \
  "ds_read_b128 v[4:7], v225 offset:1024\n" \
  "ds_read_b128 v[24:27], v225 offset:6144\n" \
  "ds_read_b128 v[8:11], v225 offset:2048\n" \
  "ds_read_b128 v[28:31], v225 offset:7168\n" \
  "ds_read_b128 v[12:15], v225 offset:3072\n" \
  "ds_read_b128 v[32:35], v225 offset:8192\n" \
  "ds_read_b128 v[16:19], v225 offset:4096\n" \
  "ds_read_b128 v[36:39], v225 offset:9216\n" \
  "s_waitcnt lgkmcnt(9)\n" \
  "v_mfma_f32_16x16x32_bf16 v[100:103], a[0:3], v[0:3], v[100:103]\n" \
  "ds_read_b128 v[40:43], v225 offset:10240\n" \
  "s_waitcnt lgkmcnt(9)\n" \
  "v_mfma_f32_16x16x32_bf16 v[104:107], a[0:3], v[20:23], v[104:107]\n" \
  "ds_read_b128 v[60:63], v225 offset:15360\n" \
  "v_mfma_f32_16x16x32_bf16 v[108:111], a[36:39], v[0:3], v[108:111]\n" \
  "ds_read_b128 v[44:47], v225 offset:11264\n" \
  "v_mfma_f32_16x16x32_bf16 v[112:115], a[36:39], v[20:23], v[112:115]\n" \
  "ds_read_b128 v[64:67], v225 offset:16384\n" \
  "s_waitcnt lgkmcnt(11)\n" \
  "v_mfma_f32_16x16x32_bf16 v[100:103], a[4:7], v[4:7], v[100:103]\n" \
  "ds_read_b128 v[48:51], v225 offset:12288\n" \
  "s_waitcnt lgkmcnt(11)\n" \
  "v_mfma_f32_16x16x32_bf16 v[104:107], a[4:7], v[24:27], v[104:107]\n" \
  "ds_read_b128 v[68:71], v225 offset:17408\n" \
  "v_mfma_f32_16x16x32_bf16 v[108:111], a[40:43], v[4:7], v[108:111]\n" \
  "ds_read_b128 v[52:55], v225 offset:13312\n" \
  "v_mfma_f32_16x16x32_bf16 v[112:115], a[40:43], v[24:27], v[112:115]\n" \
  "ds_read_b128 v[72:75], v225 offset:18432\n" \
  "s_waitcnt lgkmcnt(13)\n" \
  "v_mfma_f32_16x16x32_bf16 v[100:103], a[8:11], v[8:11], v[100:103]\n" \
  "ds_read_b128 v[56:59], v225 offset:14336\n" \
  "s_waitcnt lgkmcnt(13)\n" \
  "v_mfma_f32_16x16x32_bf16 v[104:107], a[8:11], v[28:31], v[104:107]\n" \
  "ds_read_b128 v[76:79], v225 offset:19456\n" \
  "v_mfma_f32_16x16x32_bf16 v[108:111], a[44:47], v[8:11], v[108:111]\n" \
  "ds_read_b32 v80, v224\n" \
  "v_mfma_f32_16x16x32_bf16 v[112:115], a[44:47], v[28:31], v[112:115]\n" \
  "ds_read_b32 v81, v224 offset:64\n" \
  "s_waitcnt lgkmcnt(10)\n" \
  "v_mfma_f32_16x16x32_bf16 v[100:103], a[12:15], v[12:15], v[100:103]\n" \
  "ds_read_b32 v82, v223\n" \
  "v_mfma_f32_16x16x32_bf16 v[104:107], a[12:15], v[32:35], v[104:107]\n" \
  "v_mfma_f32_16x16x32_bf16 v[108:111], a[48:51], v[12:15], v[108:111]\n" \
  "v_mfma_f32_16x16x32_bf16 v[112:115], a[48:51], v[32:35], v[112:115]\n" \
  "v_mfma_f32_16x16x32_bf16 v[100:103], a[32:35], v[16:19], v[100:103]\n" \
  "v_mfma_f32_16x16x32_bf16 v[104:107], a[32:35], v[36:39], v[104:107]\n" \
  "v_mfma_f32_16x16x32_bf16 v[108:111], a[68:71], v[16:19], v[108:111]\n" \
  "v_mfma_f32_16x16x32_bf16 v[112:115], a[68:71], v[36:39], v[112:115]\n" \
  "v_mfma_f32_16x16x32_bf16 v[192:195], a[16:19], v[40:43], 0\n" \
  "v_mfma_f32_16x16x32_bf16 v[196:199], a[16:19], v[60:63], 0\n" \
  "v_mfma_f32_16x16x32_bf16 v[200:203], a[52:55], v[40:43], 0\n" \
  "v_mfma_f32_16x16x32_bf16 v[204:207], a[52:55], v[60:63], 0\n" \
  "s_waitcnt lgkmcnt(0)\n" \
  "v_mfma_f32_16x16x32_bf16 v[192:195], a[20:23], v[44:47], v[192:195]\n" \
  "v_rcp_f32 v84, v80\n" \
  "v_rcp_f32 v86, v81\n" \
  "v_rcp_f32 v88, v82\n" \
  "v_mfma_f32_16x16x32_bf16 v[196:199], a[20:23], v[64:67], v[196:199]\n" \
  "v_mfma_f32_16x16x32_bf16 v[200:203], a[56:59], v[44:47], v[200:203]\n" \
  "v_mfma_f32_16x16x32_bf16 v[204:207], a[56:59], v[64:67], v[204:207]\n" \
  "v_mfma_f32_16x16x32_bf16 v[192:195], a[24:27], v[48:51], v[192:195]\n" \
  "v_mfma_f32_16x16x32_bf16 v[196:199], a[24:27], v[68:71], v[196:199]\n" \
  "v_mfma_f32_16x16x32_bf16 v[200:203], a[60:63], v[48:51], v[200:203]\n" \
  "v_mfma_f32_16x16x32_bf16 v[204:207], a[60:63], v[68:71], v[204:207]\n" \
  "v_mfma_f32_16x16x32_bf16 v[192:195], a[28:31], v[52:55], v[192:195]\n" \
  "v_mfma_f32_16x16x32_bf16 v[196:199], a[28:31], v[72:75], v[196:199]\n" \
  "v_mfma_f32_16x16x32_bf16 v[200:203], a[64:67], v[52:55], v[200:203]\n" \
  "v_mfma_f32_16x16x32_bf16 v[204:207], a[64:67], v[72:75], v[204:207]\n" \
  "v_mfma_f32_16x16x32_bf16 v[192:195], a[32:35], v[56:59], v[192:195]\n" \
  "v_mfma_f32_16x16x32_bf16 v[196:199], a[32:35], v[76:79], v[196:199]\n" \
  "v_mfma_f32_16x16x32_bf16 v[200:203], a[68:71], v[56:59], v[200:203]\n" \
  "v_mfma_f32_16x16x32_bf16 v[204:207], a[68:71], v[76:79], v[204:207]\n" \
  "s_cmp_ge_u32 %[w], 2\n" \
  "s_cbranch_scc1 GE%=\n" \
  "s_cmp_eq_u32 %[w], 1\n" \
  "s_cbranch_scc1 G1%=\n" \
  "v_mfma_f32_16x16x32_bf16 v[116:119], a[72:75], v[0:3], v[116:119]\n" \
  "v_mfma_f32_16x16x32_bf16 v[208:211], a[88:91], v[40:43], 0\n" \
  "v_mfma_f32_16x16x32_bf16 v[116:119], a[76:79], v[4:7], v[116:119]\n" \
  "v_mfma_f32_16x16x32_bf16 v[208:211], a[92:95], v[44:47], v[208:211]\n" \
  "v_mfma_f32_16x16x32_bf16 v[116:119], a[80:83], v[8:11], v[116:119]\n" \
  "v_mfma_f32_16x16x32_bf16 v[208:211], a[96:99], v[48:51], v[208:211]\n" \
  "v_mfma_f32_16x16x32_bf16 v[116:119], a[84:87], v[12:15], v[116:119]\n" \
  "v_mfma_f32_16x16x32_bf16 v[208:211], a[100:103], v[52:55], v[208:211]\n" \
  "v_mfma_f32_16x16x32_bf16 v[116:119], a[104:107], v[16:19], v[116:119]\n" \
  "v_mfma_f32_16x16x32_bf16 v[208:211], a[104:107], v[56:59], v[208:211]\n" \
  "s_branch GE%=\n" \
  "G1%=:\n" \
  "v_mfma_f32_16x16x32_bf16 v[116:119], a[72:75], v[20:23], v[116:119]\n" \
  "v_mfma_f32_16x16x32_bf16 v[208:211], a[88:91], v[60:63], 0\n" \
  "v_mfma_f32_16x16x32_bf16 v[116:119], a[76:79], v[24:27], v[116:119]\n" \
  "v_mfma_f32_16x16x32_bf16 v[208:211], a[92:95], v[64:67], v[208:211]\n" \
  "v_mfma_f32_16x16x32_bf16 v[116:119], a[80:83], v[28:31], v[116:119]\n" \
  "v_mfma_f32_16x16x32_bf16 v[208:211], a[96:99], v[68:71], v[208:211]\n" \
  "v_mfma_f32_16x16x32_bf16 v[116:119], a[84:87], v[32:35], v[116:119]\n" \
  "v_mfma_f32_16x16x32_bf16 v[208:211], a[100:103], v[72:75], v[208:211]\n" \
  "v_mfma_f32_16x16x32_bf16 v[116:119], a[104:107], v[36:39], v[116:119]\n" \
  "v_mfma_f32_16x16x32_bf16 v[208:211], a[104:107], v[76:79], v[208:211]\n" \
  "GE%=:\n" \
  "s_nop 4\n" \
  "v_pk_fma_f32 v[192:193], v[192:193], v[84:85], v[100:101] op_sel_hi:[1,0,1]\n" \
  "v_pk_fma_f32 v[194:195], v[194:195], v[84:85], v[102:103] op_sel_hi:[1,0,1]\n" \
  "v_pk_fma_f32 v[200:201], v[200:201], v[84:85], v[108:109] op_sel_hi:[1,0,1]\n" \
  "v_pk_fma_f32 v[202:203], v[202:203], v[84:85], v[110:111] op_sel_hi:[1,0,1]\n" \
  "v_cvt_pk_bf16_f32 v100, v192, v193\n" \
  "v_cvt_pk_bf16_f32 v101, v194, v195\n" \
  "v_cvt_pk_bf16_f32 v102, v200, v201\n" \
  "v_cvt_pk_bf16_f32 v103, v202, v203\n" \
  "global_store_dwordx4 v244, v[100:103], s[60:61]\n" \
  "v_pk_fma_f32 v[196:197], v[196:197], v[86:87], v[104:105] op_sel_hi:[1,0,1]\n" \
  "v_pk_fma_f32 v[198:199], v[198:199], v[86:87], v[106:107] op_sel_hi:[1,0,1]\n" \
  "v_pk_fma_f32 v[204:205], v[204:205], v[86:87], v[112:113] op_sel_hi:[1,0,1]\n" \
  "v_pk_fma_f32 v[206:207], v[206:207], v[86:87], v[114:115] op_sel_hi:[1,0,1]\n" \
  "v_cvt_pk_bf16_f32 v104, v196, v197\n" \
  "v_cvt_pk_bf16_f32 v105, v198, v199\n" \
  "v_cvt_pk_bf16_f32 v106, v204, v205\n" \
  "v_cvt_pk_bf16_f32 v107, v206, v207\n" \
  "global_store_dwordx4 v245, v[104:107], s[60:61]\n" \
  "s_cmp_ge_u32 %[w], 2\n" \
  "s_cbranch_scc1 END%=\n" \
  "v_pk_fma_f32 v[208:209], v[208:209], v[88:89], v[116:117] op_sel_hi:[1,0,1]\n" \
  "v_pk_fma_f32 v[210:211], v[210:211], v[88:89], v[118:119] op_sel_hi:[1,0,1]\n" \
  "v_cvt_pk_bf16_f32 v116, v208, v209\n" \
  "v_cvt_pk_bf16_f32 v117, v210, v211\n" \
  "global_store_dwordx2 v246, v[116:117], s[60:61]\n" \
  "END%=:\n"
#define MAIN_CLOB "v0","v1","v2","v3","v4","v5","v6","v7","v8","v9","v10","v11","v12","v13","v14","v15","v16","v17","v18","v19","v20","v21","v22","v23","v24","v25","v26","v27","v28","v29","v30","v31","v32","v33","v34","v35","v36","v37","v38","v39","v40","v41","v42","v43","v44","v45","v46","v47","v48","v49","v50","v51","v52","v53","v54","v55","v56","v57","v58","v59","v60","v61","v62","v63","v64","v65","v66","v67","v68","v69","v70","v71","v72","v73","v74","v75","v76","v77","v78","v79","v80","v81","v82","v83","v84","v85","v86","v87","v88","v89","v90","v91","v92","v93","v94","v95","v96","v97","v98","v99","v100","v101","v102","v103","v104","v105","v106","v107","v108","v109","v110","v111","v112","v113","v114","v115","v116","v117","v118","v119","v120","v121","v122","v123","v124","v125","v126","v127","v128","v129","v130","v131","v132","v133","v134","v135","v136","v137","v138","v139","v140","v141","v142","v143","v144","v145","v146","v147","v148","v149","v150","v151","v152","v153","v154","v155","v156","v157","v158","v159","v160","v161","v162","v163","v164","v165","v166","v167","v168","v169","v170","v171","v172","v173","v174","v175","v176","v177","v178","v179","v180","v181","v182","v183","v184","v185","v186","v187","v188","v189","v190","v191","v192","v193","v194","v195","v196","v197","v198","v199","v200","v201","v202","v203","v204","v205","v206","v207","v208","v209","v210","v211","v212","v213","v214","v215","v216","v217","v218","v219","v220","v221","v222","v223","v224","v225","v226","v227","v228","v229","v230","v231","v232","v233","v234","v235","v236","v237","v238","v239","v240","v241","v242","v243","v244","v245","v246","v247","v248","v249","v250","v251","v252","v253","v254","v255","a0","a1","a2","a3","a4","a5","a6","a7","a8","a9","a10","a11","a12","a13","a14","a15","a16","a17","a18","a19","a20","a21","a22","a23","a24","a25","a26","a27","a28","a29","a30","a31","a32","a33","a34","a35","a36","a37","a38","a39","a40","a41","a42","a43","a44","a45","a46","a47","a48","a49","a50","a51","a52","a53","a54","a55","a56","a57","a58","a59","a60","a61","a62","a63","a64","a65","a66","a67","a68","a69","a70","a71","a72","a73","a74","a75","a76","a77","a78","a79","a80","a81","a82","a83","a84","a85","a86","a87","a88","a89","a90","a91","a92","a93","a94","a95","a96","a97","a98","a99","a100","a101","a102","a103","a104","a105","a106","a107","a108","a109","a110","a111","a112","a113","a114","a115","a116","a117","a118","a119","a120","a121","a122","a123","a124","a125","a126","a127","a128","a129","a130","a131","a132","a133","a134","a135","a136","a137","a138","a139","a140","a141","a142","a143","a144","a145","a146","a147","a148","a149","a150","a151","a152","a153","a154","a155","a156","a157","a158","a159","a160","a161","a162","a163","a164","a165","a166","a167","a168","a169","a170","a171","a172","a173","a174","a175","a176","a177","a178","a179","a180","a181","a182","a183","a184","a185","a186","a187","a188","a189","a190","a191","a192","a193","a194","a195","a196","a197","a198","a199","a200","a201","a202","a203","a204","a205","a206","a207","a208","a209","a210","a211","a212","a213","a214","a215","a216","a217","a218","a219","a220","a221","a222","a223","a224","a225","a226","a227","a228","a229","a230","a231","a232","a233","a234","a235","a236","a237","a238","a239","a240","a241","a242","a243","a244","a245","a246","a247","a248","a249","a250","a251","a252","a253","a254","a255","s36","s37","s38","s39","s40","s41","s42","s43","s44","s45","s46","s47","s48","s49","s50","s51","s52","s53","s54","s55","s56","s57","s58","s59","s60","s61","s62","s63","s64","s65","s66","s67","s68","s69","s70","s71","s72","s73","s74","s75","s76","s77","s78","s79","s80","s81","s82","s83","s84","s85","s86","s87","s88","s89","s90","s91","s92","s93","s94","s95","s96","s97","s98","s99","vcc","scc","m0","memory"


DI unsigned rfl(unsigned x) { return __builtin_amdgcn_readfirstlane(x); }
DI unsigned pk(float lo, float hi) { f32x2 v = {lo, hi}; return __builtin_bit_cast(unsigned, __builtin_convertvector(v, bf16x2)); }
DI f32x4 mma(u32x4 a, u32x4 b, f32x4 c) {
  return __builtin_amdgcn_mfma_f32_16x16x32_bf16(__builtin_bit_cast(bf16x8, a), __builtin_bit_cast(bf16x8, b), c, 0, 0, 0);
}
DI float red4(float v) {
  auto a = __builtin_amdgcn_permlane16_swap(__float_as_uint(v), __float_as_uint(v), false, false);
  v = __uint_as_float(a[0]) + __uint_as_float(a[1]);
  auto b = __builtin_amdgcn_permlane32_swap(__float_as_uint(v), __float_as_uint(v), false, false);
  return __uint_as_float(b[0]) + __uint_as_float(b[1]);
}
DI u32x4 cat(u32x2 a, u32x2 b) { return (u32x4){a[0], a[1], b[0], b[1]}; }
DI u32x2 pk1(f32x4 a) { return (u32x2){pk(a[0], a[1]), pk(a[2], a[3])}; }
DI u32x4 pk2(f32x4 a, f32x4 b) { return cat(pk1(a), pk1(b)); }
DI void drain() { asm volatile("s_waitcnt vmcnt(0)" ::: "memory"); }
DI void fence_sched() { __builtin_amdgcn_sched_barrier(0); }
DI void wait_tags(const GAS u64* p, u64 tag) {
#pragma unroll 1
  for (unsigned spins = 0; spins < SPIN_CAP; ++spins) {
    const u64 v = __hip_atomic_load(p, RLX);
    if (__all(v == tag)) break;
    __builtin_amdgcn_s_sleep(8);
  }
  asm volatile("" ::: "memory");
}

struct Args { const u16 *x, *wq, *wk, *wv, *wao, *wup, *dw, *wdn, *wm; u16* out; char* ws; };

#define LAS __attribute__((address_space(3)))
typedef short s16x4 __attribute__((ext_vector_type(4)));
template <typename V> DI V ldsr(unsigned a) { return *(LAS V*)(size_t)a; }
DI u32x2 ldtr(unsigned a) { return __builtin_bit_cast(u32x2, __builtin_amdgcn_ds_read_tr16_b64_v4i16((LAS s16x4*)(size_t)a)); }
DI void dma16(__amdgpu_buffer_rsrc_t r, unsigned lds, unsigned voff) {
  asm volatile("s_nop 4\n s_mov_b32 m0, %0\n s_nop 0\n buffer_load_dwordx4 %1, %2, 0 offen lds" :: "s"(rfl(lds)), "v"(voff), "s"(r) : "m0", "memory");
}
DI __amdgpu_buffer_rsrc_t rsrc(const void* p, unsigned bytes) { return __builtin_amdgcn_make_buffer_rsrc((void*)p, 0, bytes, 0x20000); }

DI void prep_role(const Args& a, unsigned j, unsigned w) {
  const unsigned lane = __builtin_amdgcn_mbcnt_hi(-1, __builtin_amdgcn_mbcnt_lo(-1, 0)), i = lane & 15, g = lane >> 4, sw = 4 * j + w;
  const auto rws = rsrc(a.ws, WS_END);
  const unsigned pr = lane >> 1, br = (pr & 19) | ((pr & 4) << 1) | ((pr & 8) >> 1), bc = (lane & 1) * 16;
  const unsigned blk = 83968 + w * 19456, trb = blk + ((g >> 1) * 16 + (g & 1) * 4 + ((lane >> 2) & 3)) * 32 + (lane & 3) * 8;
  const bool att = j < 3;
  const unsigned n0 = att ? 3 * j + w : w, n1 = att ? (w < 3 ? n0 + 1 : 0) : 9;
  const u64 e = __hip_atomic_load((const GAS u64*)a.ws, RLX);
  { const auto ru = rsrc(a.wup, 2 * D * D * 2);
#pragma unroll
    for (int r = 0; r < 6; ++r) {
      const unsigned fi = r < 5 ? 5 * sw + r : 80, f = fi / 9, s = fi - 9 * f;
      const unsigned row = s == 8 ? 128 + (br & 15) : 32 * (s & 3) + br, gate = s == 8 ? br >> 4 : s >> 2;
      dma16(ru, blk + r * 1024, row * 576 + gate * 288 + f * 32 + bc);
    } }
  drain();
  u32x4 fr[6];
#pragma unroll
  for (int r = 0; r < 6; ++r) fr[r] = cat(ldtr(trb + r * 1024), ldtr(trb + r * 1024 + 256));
  fence_sched();
#pragma unroll
  for (int r = 0; r < 6; ++r) __builtin_amdgcn_raw_buffer_store_b128(fr[r], rws, lane * 16, r < 5 ? WS_WU + (5 * sw + r) * 1024 : (sw ? 0x7fff0000u : WS_WU + 80 * 1024), 16);
  const u64 e1 = ((u64)rfl((unsigned)(e >> 32)) << 32) | rfl((unsigned)e);
  u64 tag = (e1 ^ 0x5851F42D4C957F2DULL) * 0x9E3779B97F4A7C15ULL + 0x14057B7EF767814FULL;
  if (tag == e1) tag = ~e1;
  drain();
  if (lane == 0) __hip_atomic_store((GAS u64*)(a.ws + WS_FW + sw * FS), tag, RLX);
  { const auto r0 = rsrc(att ? a.wao : a.wdn, D * D * 2), r1 = rsrc(a.wv, att ? D * D * 2 : 0), rm = rsrc(att ? a.wm : a.wm + D * D, D * D * 2);
#pragma unroll
    for (int p = 0; p < 11; ++p) { const unsigned o = min(w + 4 * p, 40u) * 1024; dma16(r0, o, o + lane * 16); dma16(r1, 41984 + o, o + lane * 16); }
    const unsigned mo = br * 288 + bc + n0 * 32;
#pragma unroll
    for (int r = 0; r < 3; ++r) {
      const unsigned off = n0 + 4 * r < n1 ? mo + 128 * r : 0x7fff0000u;
#pragma unroll
      for (int ks = 0; ks < 5; ++ks) dma16(rm, blk + (5 * r + ks) * 1024, off + 32 * 288 * ks);
    } }
  drain();
  __syncthreads();
  u32x4 af[9][5];
#pragma unroll
  for (int t = 0; t < 9; ++t)
#pragma unroll
    for (int ks = 0; ks < 5; ++ks) af[t][ks] = ldsr<u32x4>((16 * t + i) * 288 + (ks == 4 ? 256 + 16 * (g & 1) : 64 * ks + 16 * g));
#pragma unroll 1
  for (unsigned nt = n0, r = 0; nt < n1; nt += 4, ++r) {
    u32x4 b[5]; f32x4 R[9];
#pragma unroll
    for (int ks = 0; ks < 5; ++ks) b[ks] = cat(ldtr(trb + (5 * r + ks) * 1024), ldtr(trb + (5 * r + ks) * 1024 + 256));
#pragma unroll
    for (int ks = 0; ks < 5; ++ks)
#pragma unroll
      for (int t = 0; t < 9; ++t) R[t] = mma(af[t][ks], b[ks], ks ? R[t] : (f32x4){0, 0, 0, 0});
    if (att) {
      fence_sched();
#pragma unroll
      for (int t = 0; t < 9; ++t) {
        const unsigned av = 41984 + (16 * t + i) * 288 + 8 * g;
#pragma unroll
        for (int ks = 0; ks < 5; ++ks) af[t][ks] = cat(ldsr<u32x2>(av + 64 * ks), ldsr<u32x2>(av + 64 * ks + (ks < 4 ? 32 : 0)));
      }
#pragma unroll
      for (int ks = 0; ks < 4; ++ks) b[ks] = pk2(R[2 * ks], R[2 * ks + 1]);
      b[4] = cat(pk1(R[8]), (u32x2){0, 0});
      fence_sched();
#pragma unroll
      for (int ks = 0; ks < 5; ++ks)
#pragma unroll
        for (int t = 0; t < 9; ++t) R[t] = mma(af[t][ks], b[ks], ks ? R[t] : (f32x4){0, 0, 0, 0});
      float cs = 0.f;
#pragma unroll
      for (int t = 0; t < 9; ++t) cs += (R[t][0] + R[t][1]) + (R[t][2] + R[t][3]);
      const float corr = (red4(g == 3 ? R[8][3] : 0.f) - red4(cs) * (1.f / 12.f)) * (1.f / 11.f);
#pragma unroll
      for (int t = 0; t < 9; ++t) R[t] += corr;
      if (g == 3) R[8][3] = 0.f;
    }
    const unsigned n = 16 * nt + i, o = WS_WF + (nt == 8 ? 8 * 9216 + lane * 16 : (2 * (n >> 5) + ((n >> 2) & 1)) * 9216 + (16 * g + 4 * ((n >> 3) & 3) + (n & 3)) * 16);
#pragma unroll
    for (int s = 0; s < 4; ++s) __builtin_amdgcn_raw_buffer_store_b128(pk2(R[2 * s], R[2 * s + 1]), rws, o + (att ? 4096 : 0) + s * 1024, 0, 16);
    __builtin_amdgcn_raw_buffer_store_b64(pk1(R[8]), rws, o + 8 * 1024 + (att ? 8 : 0), 0, 16);
  }
  drain();
  if (lane == 0) __hip_atomic_store((GAS u64*)(a.ws + WS_FW + (16 + sw) * FS), tag, RLX);
  if (sw == 3) {
#pragma unroll 1
    for (int k = 0; k < 4; ++k) { const unsigned q = lane + 64 * k; wait_tags((const GAS u64*)(a.ws + WS_FD + (q < 252 ? q : 0) * FS), tag); }
    wait_tags((const GAS u64*)(a.ws + WS_FW + i * FS), tag);
    if (lane == 0) __hip_atomic_store((GAS u64*)a.ws, tag, RLX);
  }
}

__global__ __launch_bounds__(256, 1) void branchformer(Args a) {
  const unsigned w = rfl(threadIdx.x >> 6), wg = blockIdx.x;
  asm volatile("s_cmp_lg_u32 %1, 1\n s_cbranch_scc1 NPF%=\n s_getpc_b64 s[60:61]\n v_and_b32 v1, 63, v0\n v_lshlrev_b32 v1, 7, v1\n v_add_u32 v1, %0, v1\n"
               "v_min_u32 v1, 35300, v1\n global_load_dword a255, v1, s[60:61]\n NPF%=:\n"
               :: "s"(wg < 252 ? ((wg >> 3) & 3) * 8192 : 27700 + (w << 11)), "s"(wg < 252 ? w : 1u) : "v1", "a255", "s60", "s61", "scc");
  if (wg >= 252) { prep_role(a, wg - 252, w); return; }
  const unsigned b = (wg >> 1) & 1, H = wg & 1, to = (wg >> 2) * 2 + H, tp = to ^ 1;
  const unsigned nb = (~(wg ^ (wg >> 3))) & 1, t0 = H * (62 + nb) + 16 * w - ((w >> 1) & w & nb), t1 = t0 + 16 - ((w >> 1) & (w | nb));
  asm volatile(MAIN_ASM
    :
    : [x] "s"(a.x), [wq] "s"(a.wq), [wk] "s"(a.wk), [dw] "s"(a.dw), [out] "s"(a.out), [ws] "s"(a.ws), [w] "s"(w), [wg] "s"(wg), [H] "s"(H),
      [bT] "s"(b * T), [tb] "s"(b * NT), [to] "s"(to), [tp] "s"(tp), [toc] "s"(min(to, NT - 1u)), [tpc] "s"(min(tp, NT - 1u)),
      [t0] "s"(t0), [t1] "s"(t1)
    : MAIN_CLOB);
}

extern "C" void kernel_launch(void* const* d_in, const int* in_sizes, int n_in, void* d_out, int out_size, void* d_ws, size_t ws_size,
                              hipStream_t stream) {
  if (n_in != 9 || in_sizes[0] != 2 * T * D || out_size != 2 * T * D || ws_size < WS_END) return;
  Args a;
  a.x = (const u16*)d_in[0]; a.wq = (const u16*)d_in[1]; a.wk = (const u16*)d_in[2]; a.wv = (const u16*)d_in[3]; a.wao = (const u16*)d_in[4];
  a.wup = (const u16*)d_in[5]; a.dw = (const u16*)d_in[6]; a.wdn = (const u16*)d_in[7]; a.wm = (const u16*)d_in[8];
  a.out = (u16*)d_out; a.ws = (char*)d_ws;
  (void)hipFuncSetAttribute((const void*)branchformer, hipFuncAttributeMaxDynamicSharedMemorySize, (int)LDS_BYTES);
  hipLaunchKernelGGL(branchformer, dim3(256), dim3(256), LDS_BYTES, stream, a);
}
```
